```python
import torch

def _softmax(x, axis=-1):
    return torch.softmax(x, dim=axis)

def _gelu(x):
    return torch.nn.functional.gelu(x, approximate='tanh')

def _silu(x):
    return torch.nn.functional.silu(x)

def _sigmoid(x):
    return torch.sigmoid(x)

def _layernorm(x, axis=-1, eps=1e-05):
    m = x.mean(dim=axis, keepdim=True)
    v = x.var(dim=axis, keepdim=True, unbiased=False)
    return (x - m) / torch.sqrt(v + eps)

def _scatter_add(tgt, idx, src, axis=0):
    return tgt.clone().index_add_(axis, idx, src)

def _segment_sum(src, seg, n):
    out = torch.zeros((n,) + src.shape[1:], dtype=src.dtype)
    return out.index_add_(0, seg, src)

def _pad(x, pw):
    flat = tuple((v for pair in reversed(pw) for v in pair))
    return torch.nn.functional.pad(x, flat)
kernel_name = 'adaLN_cond__sliding_block_cached__glu_gated_geglu__decode'

def setup_inputs():

    _w = lambda a: (lambda t: t.to(torch.bfloat16) if t.is_floating_point() else t)(torch.as_tensor(a))

    def _A_setup():
        B, Sq, D = (1, 1, 512)
        Hq, Hkv, Sk, Dh = (32, 8, 65536, 128)
        assert Hq % Hkv == 0, (Hq, Hkv)
        return {'x': _w((lambda *s: torch.randn(*s, dtype=torch.bfloat16) * 0.05)(B, Sq, D)), 'k_cache': _w((lambda *s: torch.randn(*s, dtype=torch.bfloat16) * 0.8)(B, Hkv, Sk, Dh)), 'v_cache': _w((lambda *s: torch.randn(*s, dtype=torch.bfloat16) * 3.2)(B, Hkv, Sk, Dh)), 'wq': _w((lambda *s: torch.randn(*s, dtype=torch.bfloat16) * 0.8)(D, Hq * Dh) * D ** (-0.5)), 'wo': _w((lambda *s: torch.randn(*s, dtype=torch.bfloat16) * 3.2)(Hq * Dh, D) * (Hq * Dh) ** (-0.5))}
    _A_in = _A_setup()

    def _B_setup():
        B, S, Dm, Df = (1, 1, 512, 1792)
        return {'x': _w((lambda *s: torch.randn(*s, dtype=torch.bfloat16) * 0.05)(B, S, Dm)), 'w_gate': _w((lambda *s: torch.randn(*s, dtype=torch.bfloat16) * 3.2)(Dm, Df) * Dm ** (-0.5)), 'w_up': _w((lambda *s: torch.randn(*s, dtype=torch.bfloat16) * 3.2)(Dm, Df) * Dm ** (-0.5)), 'w_down': _w((lambda *s: torch.randn(*s, dtype=torch.bfloat16) * 3.2)(Df, Dm) * Df ** (-0.5))}
    _B_in = _B_setup()
    return {'A_x': _A_in['x'], 'A_k_cache': _A_in['k_cache'], 'A_v_cache': _A_in['v_cache'], 'A_wq': _A_in['wq'], 'A_wo': _A_in['wo'], 'B_x': _B_in['x'], 'B_w_gate': _B_in['w_gate'], 'B_w_up': _B_in['w_up'], 'B_w_down': _B_in['w_down'], 'c': _w((lambda *s: torch.randn(*s, dtype=torch.bfloat16) * 0.4)(1, 512) * 8.0), 'w_mod': _w((lambda *s: torch.randn(*s, dtype=torch.bfloat16) * 0.4)(512, 6 * 512) * 512 ** (-0.5) * 8.0)}

def reference(A_x, A_k_cache, A_v_cache, A_wq, A_wo, B_x, B_w_gate, B_w_up, B_w_down, c, w_mod):

    def _A_ref(x, k_cache, v_cache, wq, wo):
        B, Sq, D = x.shape
        Hq, Dh = (32, 128)
        Hkv, Sk = (k_cache.shape[1], k_cache.shape[2])
        W = min(4096, max(256, Sk // 4))
        rep = Hq // Hkv
        q = (lambda x, *d: x.permute(*d))((x @ wq).reshape(B, Sq, Hq, Dh), 0, 2, 1, 3)
        k = (lambda x, n, axis: torch.repeat_interleave(x, n, dim=axis))(k_cache[:, :, Sk - W:, :], rep, axis=1)
        v = (lambda x, n, axis: torch.repeat_interleave(x, n, dim=axis))(v_cache[:, :, Sk - W:, :], rep, axis=1)
        scores = torch.einsum('bhqd,bhkd->bhqk', q, k) * Dh ** (-0.5)
        p = _softmax(scores, axis=-1)
        o = torch.einsum('bhqk,bhkd->bhqd', p, v)
        return (lambda x, *d: x.permute(*d))(o, 0, 2, 1, 3).reshape(B, Sq, Hq * Dh) @ wo

    def _B_ref(x, w_gate, w_up, w_down):
        g = torch.einsum('bsd,df->bsf', x, w_gate)
        u = torch.einsum('bsd,df->bsf', x, w_up)
        h = _gelu(g) * u
        return torch.einsum('bsf,fd->bsd', h, w_down)
    x = A_x
    mod = (_silu(c) @ w_mod).reshape(c.shape[0], 6, 1, -1)
    sa, ca, ga, sb, cb, gb = (mod[:, 0].clone(), mod[:, 1].clone(), mod[:, 2].clone(), mod[:, 3].clone(), mod[:, 4].clone(), mod[:, 5].clone())
    h = _layernorm(x, axis=-1) * (1 + ca) + sa
    x = x + ga * _A_ref(h, A_k_cache, A_v_cache, A_wq, A_wo)
    h = _layernorm(x, axis=-1) * (1 + cb) + sb
    x = x + gb * _B_ref(h, B_w_gate, B_w_up, B_w_down)
    return x
```

```cpp
#include <hip/hip_runtime.h>
#include <hip/hip_bf16.h>
#include <stdint.h>

typedef unsigned short u16;
__device__ __forceinline__ float bf2f(u16 v) { return __uint_as_float(((unsigned)v) << 16); }
__device__ __forceinline__ float rbf(float f) {
  unsigned u = __float_as_uint(f);
  u = (u + 0x7fffu + ((u >> 16) & 1u)) & 0xffff0000u;
  return __uint_as_float(u);
}
__device__ __forceinline__ u16 f2bf(float f) {
  unsigned u = __float_as_uint(f);
  return (u16)((u + 0x7fffu + ((u >> 16) & 1u)) >> 16);
}

constexpr int D = 512, HQ = 32, HKV = 8, SK = 65536, DH = 128, W = 4096, DF = 1792;

__global__ void k_mod(const u16* c, const u16* wmod, float* mod) {
  __shared__ float s[512];
  for (int i = threadIdx.x; i < 512; i += blockDim.x) { float v = bf2f(c[i]); s[i] = rbf(v / (1.f + __expf(-v))); }
  __syncthreads();
  int n = blockIdx.x * blockDim.x + threadIdx.x;
  float acc = 0.f;
  for (int k = 0; k < 512; ++k) acc += s[k] * bf2f(wmod[(size_t)k * 3072 + n]);
  mod[n] = rbf(acc);
}
__device__ void ln_mod(const float* xin, const float* sc, const float* sh, float* h  , float* red) {
  int t = threadIdx.x;
  float a = xin[t], b = xin[t + 256];
  red[t] = a + b; __syncthreads();
  for (int o = 128; o > 0; o >>= 1) { if (t < o) red[t] += red[t + o]; __syncthreads(); }
  float meanf = red[0] / 512.f; __syncthreads();
  float m = rbf(meanf);
  red[t] = (a - meanf) * (a - meanf) + (b - meanf) * (b - meanf); __syncthreads();
  for (int o = 128; o > 0; o >>= 1) { if (t < o) red[t] += red[t + o]; __syncthreads(); }
  float v = rbf(red[0] / 512.f); __syncthreads();
  float den = rbf(sqrtf(rbf(v + 1e-5f)));
  for (int i = t; i < 512; i += 256) {
    float xn = rbf(rbf(xin[i] - m) / den);
    h[i] = rbf(rbf(xn * rbf(1.f + sc[i])) + sh[i]);
  }
  __syncthreads();
}
__global__ void k_q(const u16* x, const float* mod, const u16* wq, float* q) {
  __shared__ float xs[512], h[512], red[256];
  for (int i = threadIdx.x; i < 512; i += 256) xs[i] = bf2f(x[i]);
  __syncthreads();
  ln_mod(xs, mod + 512, mod, h, red);
  int n = blockIdx.x * 256 + threadIdx.x;
  float acc = 0.f;
  for (int k = 0; k < 512; ++k) acc += h[k] * bf2f(wq[(size_t)k * 4096 + n]);
  q[n] = rbf(acc);
}
__global__ void __launch_bounds__(128) k_attn(const float* q, const u16* kc, const u16* vc, float* po, float* pml) {
  int hq = blockIdx.y, sp = blockIdx.x; int g = hq / 4;
  __shared__ float qs[128], p[128], red[2];
  int t = threadIdx.x;
  qs[t] = q[hq * 128 + t];
  __syncthreads();
  const u16* kr = kc + ((size_t)g * SK + (SK - W) + sp * 128 + t) * DH;
  float acc = 0.f;
  for (int d = 0; d < 128; ++d) acc += qs[d] * bf2f(kr[d]);
  float s = rbf(rbf(acc) * 0.08838834764831845f);
  p[t] = s; __syncthreads();
  float m = -INFINITY;
  for (int i = 0; i < 128; ++i) m = fmaxf(m, p[i]);
  __syncthreads();
  float e = __expf(s - m);
  p[t] = e; __syncthreads();
  float l = 0.f; for (int i = 0; i < 128; ++i) l += p[i];
  float o = 0.f;
  const u16* vr = vc + ((size_t)g * SK + (SK - W) + sp * 128) * DH + t;
  for (int i = 0; i < 128; ++i) o += p[i] * bf2f(vr[(size_t)i * DH]);
  po[((size_t)hq * 32 + sp) * 128 + t] = o;
  if (t == 0) { pml[(hq * 32 + sp) * 2] = m; pml[(hq * 32 + sp) * 2 + 1] = l; }
}
__global__ void k_comb(const float* po, const float* pml, float* o) {
  int hq = blockIdx.x; int t = threadIdx.x;
  float M = -INFINITY;
  for (int s = 0; s < 32; ++s) M = fmaxf(M, pml[(hq * 32 + s) * 2]);
  float L = 0.f, acc = 0.f;
  for (int s = 0; s < 32; ++s) { float w = __expf(pml[(hq * 32 + s) * 2] - M); L += w * pml[(hq * 32 + s) * 2 + 1]; acc += w * po[((size_t)hq * 32 + s) * 128 + t]; }
  o[hq * 128 + t] = rbf(acc / L);
}
__global__ void k_wo(const float* o, const u16* wo, float* part) {
  int ks = blockIdx.x; int n = blockIdx.y * 256 + threadIdx.x;
  float acc = 0.f;
  for (int k = ks * 128; k < ks * 128 + 128; ++k) acc += o[k] * bf2f(wo[(size_t)k * 512 + n]);
  part[ks * 512 + n] = acc;
}
__global__ void k_gu(const u16* x, const float* mod, const float* part, const u16* wg, const u16* wu, float* hh, float* x1out) {
  __shared__ float xs[512], h[512], red[256];
  for (int i = threadIdx.x; i < 512; i += 256) {
    float a = 0.f; for (int s = 0; s < 32; ++s) a += part[s * 512 + i];
    float x1 = rbf(bf2f(x[i]) + rbf(mod[1024 + i] * rbf(a)));
    xs[i] = x1; if (blockIdx.x == 0) x1out[i] = x1;
  }
  __syncthreads();
  ln_mod(xs, mod + 4 * 512, mod + 3 * 512, h, red);
  int n = blockIdx.x * 256 + threadIdx.x;
  float g = 0.f, u = 0.f;
  for (int k = 0; k < 512; ++k) { g += h[k] * bf2f(wg[(size_t)k * DF + n]); u += h[k] * bf2f(wu[(size_t)k * DF + n]); }
  g = rbf(g); u = rbf(u);
  float inner = 0.7978845608028654f * (g + 0.044715f * g * g * g);
  float ge = rbf(0.5f * g * (1.f + tanhf(inner)));
  hh[n] = rbf(ge * u);
}
__global__ void k_down(const float* hh, const u16* wd, float* part) {
  int ks = blockIdx.x; int n = blockIdx.y * 256 + threadIdx.x;
  float acc = 0.f;
  for (int k = ks * 64; k < ks * 64 + 64; ++k) acc += hh[k] * bf2f(wd[(size_t)k * 512 + n]);
  part[ks * 512 + n] = acc;
}
__global__ void k_fin(const float* x1, const float* mod, const float* part, u16* out) {
  int n = blockIdx.x * 256 + threadIdx.x;
  float a = 0.f; for (int s = 0; s < 28; ++s) a += part[s * 512 + n];
  out[n] = f2bf(x1[n] + rbf(mod[5 * 512 + n] * rbf(a)));
}

extern "C" void kernel_launch(void* const* d_in, const int* in_sizes, int n_in,
                              void* d_out, int out_size, void* d_ws, size_t ws_size,
                              hipStream_t stream) {
  const u16* x = (const u16*)d_in[0];
  const u16* kc = (const u16*)d_in[1];
  const u16* vc = (const u16*)d_in[2];
  const u16* wq = (const u16*)d_in[3];
  const u16* wo = (const u16*)d_in[4];
  const u16* wg = (const u16*)d_in[6];
  const u16* wu = (const u16*)d_in[7];
  const u16* wd = (const u16*)d_in[8];
  const u16* c = (const u16*)d_in[9];
  const u16* wmod = (const u16*)d_in[10];
  float* ws = (float*)d_ws;
  float* mod = ws; float* q = ws + 4096; float* po = ws + 16384; float* pml = po + 32 * 32 * 128; float* o = pml + 4096;
  float* part = o + 4096; float* hh = part + 32 * 512; float* x1 = hh + 2048; float* part2 = x1 + 512;
  k_mod<<<12, 256, 0, stream>>>(c, wmod, mod);
  k_q<<<16, 256, 0, stream>>>(x, mod, wq, q);
  k_attn<<<dim3(32, 32), 128, 0, stream>>>(q, kc, vc, po, pml);
  k_comb<<<32, 128, 0, stream>>>(po, pml, o);
  k_wo<<<dim3(32, 2), 256, 0, stream>>>(o, wo, part);
  k_gu<<<7, 256, 0, stream>>>(x, mod, part, wg, wu, hh, x1);
  k_down<<<dim3(28, 2), 256, 0, stream>>>(hh, wd, part2);
  k_fin<<<2, 256, 0, stream>>>(x1, mod, part2, (u16*)d_out);
}
```

```cpp
#include <hip/hip_runtime.h>
#include <stdint.h>

typedef unsigned int u32;
typedef unsigned short u16;
typedef u32 v4u __attribute__((ext_vector_type(4)));
typedef u32 v2u __attribute__((ext_vector_type(2)));
typedef float v4f __attribute__((ext_vector_type(4)));
typedef float v2f __attribute__((ext_vector_type(2)));
typedef __bf16 v8bf __attribute__((ext_vector_type(8)));
typedef __bf16 v2bf __attribute__((ext_vector_type(2)));
typedef short v4s __attribute__((ext_vector_type(4)));
typedef __amdgpu_buffer_rsrc_t rsrc_t;
#define DI __device__ __forceinline__

#define PROF 0

constexpr u32 WS_EP = 0;
constexpr u32 WS_H1 = 4096;
constexpr u32 WS_H2 = WS_H1 + 32768;
constexpr u32 WS_ST = WS_H2 + 65536;
constexpr u32 WS_MR = WS_ST + 16384;
constexpr u32 WS_H4 = WS_MR + 32768;
constexpr u32 WS_H3 = WS_H4 + 32768;
constexpr u32 WS_H5 = WS_H3 + 262144;
constexpr u32 WS_END = WS_H5 + 112 * 2048;
constexpr u32 WS_PROF = 1u << 20;

constexpr int AUX_NT = 2, AUX_SC1 = 16;
constexpr u32 SPIN_MAX = 1u << 16;

#define DLY_H1 0
#define DLY_H3 0
#define DLY_H4 0
#define DLY_H5 0

DI rsrc_t mk(const void* p, u32 bytes) { return __builtin_amdgcn_make_buffer_rsrc((void*)p, 0, bytes, 0x00020000); }
template <int AUX> DI v4u ld128(rsrc_t r, u32 vo, u32 so) { return __builtin_amdgcn_raw_buffer_load_b128(r, vo, so, AUX); }
template <int AUX> DI u32 ld32(rsrc_t r, u32 vo, u32 so) { return __builtin_amdgcn_raw_buffer_load_b32(r, vo, so, AUX); }
DI u32 ld16(rsrc_t r, u32 vo, u32 so) { return (u32)(u16)__builtin_amdgcn_raw_buffer_load_b16(r, vo, so, 0); }
DI void st32(rsrc_t r, u32 v, u32 vo, u32 so) { __builtin_amdgcn_raw_buffer_store_b32(v, r, vo, so, AUX_SC1); }
DI void st128(rsrc_t r, v4u v, u32 vo, u32 so) { __builtin_amdgcn_raw_buffer_store_b128(v, r, vo, so, AUX_SC1); }
DI float asf(u32 x) { return __uint_as_float(x); }
DI u32 asu(float x) { return __float_as_uint(x); }
DI u32 cvtpk(float lo, float hi) { v2f v = {lo, hi}; return __builtin_bit_cast(u32, __builtin_convertvector(v, v2bf)); }
DI u32 dup(float x) { return cvtpk(x, x); }
DI float rbf(float x) { return asf(dup(x) & 0xffff0000u); }
template <int C> DI u32 dppu(u32 x) { return (u32)__builtin_amdgcn_update_dpp(0, (int)x, C, 0xf, 0xf, true); }
template <int C> DI float dppf(float x) { return asf(dppu<C>(asu(x))); }
constexpr int ROR8 = 0x128, ROR4 = 0x124, ROR2 = 0x122, ROR1 = 0x121;
constexpr int QP(int a, int b, int c, int d) { return a | (b << 2) | (c << 4) | (d << 6); }
DI float sw32add(float a, float b) { auto r = __builtin_amdgcn_permlane32_swap(asu(a), asu(b), false, false); return asf(r[0]) + asf(r[1]); }
DI float sw16add(float a, float b) { auto r = __builtin_amdgcn_permlane16_swap(asu(a), asu(b), false, false); return asf(r[0]) + asf(r[1]); }
DI float sw32max(float a) { auto r = __builtin_amdgcn_permlane32_swap(asu(a), asu(a), false, false); return fmaxf(asf(r[0]), asf(r[1])); }
DI float sw16max(float a) { auto r = __builtin_amdgcn_permlane16_swap(asu(a), asu(a), false, false); return fmaxf(asf(r[0]), asf(r[1])); }
DI float tred4(float x, float y, float z, float w) { return sw16add(sw32add(x, z), sw32add(y, w)); }
DI float rowsum(float v) { v += dppf<ROR8>(v); v += dppf<ROR4>(v); v += dppf<ROR2>(v); v += dppf<ROR1>(v); return v; }
DI float rowmax(float v) { v = fmaxf(v, dppf<ROR8>(v)); v = fmaxf(v, dppf<ROR4>(v)); v = fmaxf(v, dppf<ROR2>(v)); v = fmaxf(v, dppf<ROR1>(v)); return v; }
DI float wavesum(float v) { v = rowsum(v); v = sw16add(v, v); return sw32add(v, v); }
DI u32 enc(float v, u32 T) { return (asu(v) & 0xffffff00u) | T; }
DI void cfence() { asm volatile("" ::: "memory"); }
DI void bar() { asm volatile("s_waitcnt lgkmcnt(0)\n\ts_barrier" ::: "memory"); }
DI v4f mfma32(v4u a, v4u b, v4f c) { return __builtin_amdgcn_mfma_f32_16x16x32_bf16(__builtin_bit_cast(v8bf, a), __builtin_bit_cast(v8bf, b), c, 0, 0, 0); }
DI v4f mfma16(v2u a, v2u b, v4f c) { return __builtin_amdgcn_mfma_f32_16x16x16bf16_1k(__builtin_bit_cast(v4s, a), __builtin_bit_cast(v4s, b), c, 0, 0, 0); }
DI float silu_bf(u32 h) { float v = asf(h << 16); return v * __builtin_amdgcn_rcpf(1.f + __builtin_amdgcn_exp2f(-1.4426950408889634f * v)); }
DI float dot2(u32 a, u32 b, float c) { return __builtin_amdgcn_fdot2_f32_bf16(__builtin_bit_cast(v2bf, a), __builtin_bit_cast(v2bf, b), c, false); }
template <int N> DI void slp() { if (N > 0) __builtin_amdgcn_s_sleep(N); }

DI u32 untag(v4u& y, u32 T) { y.x ^= T; y.y ^= T; y.z ^= T; y.w ^= T; return y.x | y.y | y.z | y.w; }
template <int N> DI void gather(v4u (&y)[N], rsrc_t ws, const u32 (&vo)[N], u32 so, u32 T, bool have) {
  if (__builtin_amdgcn_ballot_w64(!have) == 0) return;
#pragma unroll 1
  for (u32 spin = 0; spin < SPIN_MAX; ++spin) {
    cfence();
#pragma unroll
    for (int k = 0; k < N; ++k) y[k] = ld128<AUX_SC1>(ws, vo[k], so);
    u32 bad = 0;
#pragma unroll
    for (int k = 0; k < N; ++k) bad |= untag(y[k], T);
    if (__builtin_amdgcn_ballot_w64((bad & 255u) != 0) == 0) break;
  }
}

#define STAMP(k) do {} while (0)

constexpr u32 L_RA = 0, L_RB = 2048, L_RM = 6144, L_RD = 8192;
constexpr u32 L_Q = 0, L_ML = 1024, L_X1 = 1536, L_H2 = 2560, L_G = 3584, L_RF = 4096, L_O = 8192, L_W = 24576;
constexpr u32 LDS_BYTES = 84 * 1024;

__global__ void __launch_bounds__(512, 2)
fused(const u16* __restrict__ xin, const u16* __restrict__ kc, const u16* __restrict__ vc, const u16* __restrict__ wq,
      const u16* __restrict__ wo, const u16* __restrict__ wg, const u16* __restrict__ wu, const u16* __restrict__ wd,
      const u16* __restrict__ cin, const u16* __restrict__ wmod, u16* __restrict__ out, char* wsp) {
  __shared__ __attribute__((aligned(16))) char lds[LDS_BYTES];
  STAMP(0);
  const int tid = threadIdx.x;
  const int w = __builtin_amdgcn_readfirstlane(tid >> 6);
  const int l = tid & 63, g = l >> 4, c = l & 15;
  const int xcd = blockIdx.x & 7, role = (blockIdx.x >> 3) & 1, i = blockIdx.x >> 4;
  const int idx = xcd * 16 + i;
  const rsrc_t ws = mk(wsp, WS_END);
  const rsrc_t rx = mk(xin, 1024);
  const u32 ep = ld32<AUX_SC1>(ws, 0, WS_EP);
  const int jj = (c & 3) + 4 * (c >> 3);
  const u32 hm = (jj & 1) ? 0xffff0000u : 0x0000ffffu;
  const v4u MK = {(jj >> 1) == 0 ? hm : 0u, (jj >> 1) == 1 ? hm : 0u, (jj >> 1) == 2 ? hm : 0u, (jj >> 1) == 3 ? hm : 0u};
  const v4f Z4 = {0.f, 0.f, 0.f, 0.f};
  float* ldsf = (float*)lds;

  if (role == 0) {
    const rsrc_t rmod = mk(wmod, 512 * 3072 * 2), rc = mk(cin, 1024), rwq = mk(wq, 512 * 4096 * 2), rwo = mk(wo, 4096 * 512 * 2);
    const int ka = idx >> 4, atc = idx & 15, kb = idx >> 5, head = idx & 31;
    const int a = (l >> 2) & 1;
    const u32 rowsel = 4 * (c >> 3) + g;
    const u32 cA = ld16(rc, (4 * a + g) * 2, (64 * ka + 8 * w) * 2);
    const v4u wA = ld128<AUX_NT>(rmod, rowsel * 6144 + (c & 7) * 16, (64 * ka + 8 * w) * 6144 + atc * 128);
    const v4u xv = ld128<0>(rx, l * 16, 0);
    const u32 xo = ld16(rx, (4 * (l & 3) + g) * 2, (128 * kb + 16 * w) * 2);
    const u32 cM0 = ld16(rc, (4 * a + g) * 2, (128 * kb + 16 * w) * 2);
    const u32 cM1 = ld16(rc, (4 * a + g) * 2 + 16, (128 * kb + 16 * w) * 2);
    cfence();
    v4u wB[4];
#pragma unroll
    for (int t = 0; t < 4; ++t) wB[t] = ld128<AUX_NT>(rwq, g * 8192 + c * 16, (128 * kb + 16 * w + 4 * t) * 8192 + head * 256);
    cfence();
    const u32 T = ((u32)__builtin_amdgcn_readfirstlane(ep) + 1u) & 255u;
    {
      const u32 sd = dup(silu_bf(cA));
      const v4f acc = mfma32(MK & sd, wA, Z4);
      auto p0 = __builtin_amdgcn_permlane16_swap(asu(acc.x), asu(acc.y), false, false);
      auto p1 = __builtin_amdgcn_permlane16_swap(asu(acc.z), asu(acc.w), false, false);
      const float s0 = asf(p0[0]) + dppf<ROR8>(asf(p0[1])), s1 = asf(p1[0]) + dppf<ROR8>(asf(p1[1]));
      if (c < 8) { float* p = ldsf + L_RA / 4 + w * 64 + 8 * c + (g & 1) + 4 * (g >> 1); p[0] = s0; p[2] = s1; }
    }
    STAMP(1);
    bar();
    if (w == 0) {
      const float* p = ldsf + L_RA / 4 + l;
      const float s = ((p[0] + p[64]) + (p[128] + p[192])) + ((p[256] + p[320]) + (p[384] + p[448]));
      st32(ws, enc(s, T), l * 4, WS_H1 + (ka * 16 + atc) * 256);
    }
    STAMP(2);
    float xn;
    {
      const u32 ONE = 0x3f803f80u;
      float s = dot2(xv.x, ONE, dot2(xv.y, ONE, 0.f)) + dot2(xv.z, ONE, dot2(xv.w, ONE, 0.f));
      const float mean = wavesum(s) * (1.f / 512.f);
      float q = 0.f;
      const u32 xs[4] = {xv.x, xv.y, xv.z, xv.w};
#pragma unroll
      for (int k = 0; k < 4; ++k) { const float d0 = asf(xs[k] << 16) - mean, d1 = asf(xs[k] & 0xffff0000u) - mean; q = fmaf(d0, d0, fmaf(d1, d1, q)); }
      const float var = rbf(wavesum(q) * (1.f / 512.f));
      const float den = rbf(__builtin_amdgcn_sqrtf(rbf(var + 1e-5f)));
      xn = rbf(rbf(asf(xo << 16) - rbf(mean)) * __builtin_amdgcn_rcpf(den));
    }
    const u32 sM0 = dup(silu_bf(cM0)), sM1 = dup(silu_bf(cM1));
    {
      slp<DLY_H1>();
      v4u y[1];
      const u32 vo[1] = {(u32)((l >> 2) * 2048 + (l & 3) * 16)};
      gather<1>(y, ws, vo, WS_H1 + (128 * kb + 16 * w) * 4, T, false);
      STAMP(3);
      float v = tred4(asf(y[0].x), asf(y[0].y), asf(y[0].z), asf(y[0].w));
      v = rbf(v + dppf<ROR8>(v));
      const float o = dppf<ROR4>(v);
      const float sa = a ? o : v, ca = a ? v : o;
      const u32 hd = dup(rbf(xn * rbf(1.f + ca)) + sa);
      v4f acc = Z4;
      acc = mfma32(MK & dppu<QP(0, 0, 0, 0)>(hd), wB[0], acc);
      acc = mfma32(MK & dppu<QP(1, 1, 1, 1)>(hd), wB[1], acc);
      acc = mfma32(MK & dppu<QP(2, 2, 2, 2)>(hd), wB[2], acc);
      acc = mfma32(MK & dppu<QP(3, 3, 3, 3)>(hd), wB[3], acc);
      if ((g & 1) == 0) *(v4f*)(ldsf + L_RB / 4 + w * 128 + 8 * c + 2 * g) = acc;
    }
    STAMP(4);
    bar();
    if (w < 2) {
      const float* p = ldsf + L_RB / 4 + tid;
      const float s = ((p[0] + p[128]) + (p[256] + p[384])) + ((p[512] + p[640]) + (p[768] + p[896]));
      st32(ws, enc(s, T), tid * 4, WS_H2 + kb * 16384 + head * 512);
    }
    asm volatile("s_barrier" ::: "memory");
    STAMP(5);
    const int I = idx >> 3, J = idx & 7;
    v4u wM[2], wD[4];
#pragma unroll
    for (int t = 0; t < 2; ++t) wM[t] = ld128<AUX_NT>(rmod, rowsel * 6144 + (c & 7) * 16, (128 * kb + 16 * w + 8 * t) * 6144 + 2048 + head * 128);
#pragma unroll
    for (int t = 0; t < 4; ++t) wD[t] = ld128<AUX_NT>(rwo, (16 * (c >> 3) + g) * 1024 + (c & 7) * 16, (256 * I + 32 * w + 4 * t) * 1024 + J * 128);
    cfence();
    u32 pf;
    {
      const rsrc_t rf = mk((w & 4) ? wu : wg, 512 * 1792 * 2);
      pf = ld32<0>(rf, (l >> 2) * 3584 + (l & 3) * 128, (i * 32 + (w & 1) * 16) * 3584 + ((xcd * 448) & ~127));
      if (w & 2) pf = 0;
    }
    cfence();
    {
      v4f acc = mfma32(MK & sM0, wM[0], Z4);
      acc = mfma32(MK & sM1, wM[1], acc);
      auto p0 = __builtin_amdgcn_permlane16_swap(asu(acc.x), asu(acc.y), false, false);
      auto p1 = __builtin_amdgcn_permlane16_swap(asu(acc.z), asu(acc.w), false, false);
      const float s0 = asf(p0[0]) + dppf<ROR8>(asf(p0[1])), s1 = asf(p1[0]) + dppf<ROR8>(asf(p1[1]));
      if (c < 8) { float* p = ldsf + L_RM / 4 + w * 64 + 8 * c + (g & 1) + 4 * (g >> 1); p[0] = s0; p[2] = s1; }
    }
    bar();
    if (w == 1) {
      const float* p = ldsf + L_RM / 4 + l;
      const float s = ((p[0] + p[64]) + (p[128] + p[192])) + ((p[256] + p[320]) + (p[384] + p[448]));
      st32(ws, enc(s, T), l * 4, WS_MR + kb * 8192 + head * 256);
    }
    STAMP(6);
    const int gk = I >> 1, hl = 2 * (I & 1) + (w >> 2);
    float f0, f1;
    {
      v4u y[1];
      const u32 vo[1] = {(u32)(c * 128)};
      gather<1>(y, ws, vo, WS_ST + gk * 2048 + hl * 16, T, false);
      const float m = asf(y[0].x), M = rowmax(m);
      const float e = __builtin_amdgcn_exp2f(m - M);
      const float f = e * __builtin_amdgcn_rcpf(rowsum(e * asf(y[0].y)));
      f0 = asf(__builtin_amdgcn_ds_bpermute((l >> 3) * 4, asu(f)));
      f1 = asf(__builtin_amdgcn_ds_bpermute((l >> 3) * 4 + 32, asu(f)));
    }
    STAMP(7);
    float ga = 0.f;
    if (w == 0) {
      v4u y[1];
      const u32 vo[1] = {(u32)(g * 8192 + c * 16)};
      gather<1>(y, ws, vo, WS_MR + J * 256, T, false);
      ga = rbf(tred4(asf(y[0].x), asf(y[0].y), asf(y[0].z), asf(y[0].w)));
    }
    STAMP(8);
    {
      slp<DLY_H3>();
      v4u y[2];
      const u32 vo[2] = {(u32)((l >> 3) * 2048 + (l & 7) * 16), (u32)((l >> 3) * 2048 + (l & 7) * 16 + 16384)};
      gather<2>(y, ws, vo, WS_H3 + gk * 32768 + hl * 512 + (w & 3) * 128, T, false);
      STAMP(9);
      float v = tred4(fmaf(f0, asf(y[0].x), f1 * asf(y[1].x)), fmaf(f0, asf(y[0].y), f1 * asf(y[1].y)),
                      fmaf(f0, asf(y[0].z), f1 * asf(y[1].z)), fmaf(f0, asf(y[0].w), f1 * asf(y[1].w)));
      const u32 od = dup(v + dppf<ROR8>(v));
      v4f acc = Z4;
      acc = mfma32(MK & dppu<QP(0, 0, 0, 0)>(od), wD[0], acc);
      acc = mfma32(MK & dppu<QP(1, 1, 1, 1)>(od), wD[1], acc);
      acc = mfma32(MK & dppu<QP(2, 2, 2, 2)>(od), wD[2], acc);
      acc = mfma32(MK & dppu<QP(3, 3, 3, 3)>(od), wD[3], acc);
      auto p0 = __builtin_amdgcn_permlane16_swap(asu(acc.x), asu(acc.y), false, false);
      auto p1 = __builtin_amdgcn_permlane16_swap(asu(acc.z), asu(acc.w), false, false);
      const float s0 = asf(p0[0]) + dppf<ROR8>(asf(p0[1])), s1 = asf(p1[0]) + dppf<ROR8>(asf(p1[1]));
      if (c < 8) { float* p = ldsf + L_RD / 4 + w * 64 + 8 * c + (g & 1) + 4 * (g >> 1); p[0] = s0; p[2] = s1; }
    }
    bar();
    if (w == 0) {
      const float* p = ldsf + L_RD / 4 + 4 * c + g;
      const float s = ((p[0] + p[64]) + (p[128] + p[192])) + ((p[256] + p[320]) + (p[384] + p[448]));
      st32(ws, enc(s * ga, T), (4 * c + g) * 4, WS_H4 + I * 2048 + J * 256);
    }
    STAMP(10);
    asm volatile("" :: "v"(pf));
  } else {
    const rsrc_t rk = mk(kc + ((size_t)xcd * 65536 + 61440) * 128, 4096 * 256), rv = mk(vc + ((size_t)xcd * 65536 + 61440) * 128, 4096 * 256);
    const int rot = w + i + xcd;
    const u32 voK = (c & 7) * 256 + (2 * g + (c >> 3)) * 16, voV = (4 * (g & 1) + (g >> 1)) * 256 + c * 16;
    const u32 sb0 = i * 65536 + w * 8192;
    v4u kr[4][2], vr[4][2];
#pragma unroll
    for (int q = 0; q < 4; ++q) {
      kr[q][0] = ld128<AUX_NT>(rk, voK, sb0 + ((q + rot) & 3) * 2048);
      kr[q][1] = ld128<AUX_NT>(rk, voK + 128, sb0 + ((q + rot) & 3) * 2048);
    }
    cfence();
#pragma unroll
    for (int q = 0; q < 2; ++q) {
      vr[q][0] = ld128<AUX_NT>(rv, voV, sb0 + ((q + rot) & 3) * 2048);
      vr[q][1] = ld128<AUX_NT>(rv, voV + 512, sb0 + ((q + rot) & 3) * 2048);
    }
    asm volatile("s_waitcnt vmcnt(4)" ::: "memory");
    STAMP(1);
    const u32 T = ((u32)__builtin_amdgcn_readfirstlane(ep) + 1u) & 255u;
    {
      v4u y[1];
      const u32 vo[1] = {(u32)(g * 16384 + c * 16)};
      y[0] = ld128<AUX_SC1>(ws, vo[0], WS_H2 + xcd * 2048 + w * 256);
      cfence();
#pragma unroll
      for (int q = 2; q < 4; ++q) {
        vr[q][0] = ld128<AUX_NT>(rv, voV, sb0 + ((q + rot) & 3) * 2048);
        vr[q][1] = ld128<AUX_NT>(rv, voV + 512, sb0 + ((q + rot) & 3) * 2048);
      }
      cfence();
      const bool ok = (untag(y[0], T) & 255u) == 0;
      STAMP(2);
      gather<1>(y, ws, vo, WS_H2 + xcd * 2048 + w * 256, T, ok);
      STAMP(3);
      const float qv = tred4(asf(y[0].x), asf(y[0].y), asf(y[0].z), asf(y[0].w));
      *(u16*)(lds + L_Q + (64 * w + 4 * c + g) * 2) = (u16)dup(qv);
    }
    bar();
    float sc[8];
    {
      const v4u q0 = *(const v4u*)(lds + L_Q + (c >> 2) * 256 + (2 * g + (c & 1)) * 16);
      const v4u q1 = *(const v4u*)(lds + L_Q + (c >> 2) * 256 + (2 * g + (c & 1)) * 16 + 128);
#pragma unroll
      for (int q = 0; q < 4; ++q) {
        const v4f d = mfma32(kr[q][1], q1, mfma32(kr[q][0], q0, Z4));
        auto p0 = __builtin_amdgcn_permlane32_swap(asu(d.x), asu(d.y), false, false);
        auto p1 = __builtin_amdgcn_permlane32_swap(asu(d.z), asu(d.w), false, false);
        const float s0 = asf(p0[0]) + dppf<QP(1, 1, 1, 1)>(asf(p0[1])), s1 = asf(p1[0]) + dppf<QP(1, 1, 1, 1)>(asf(p1[1]));
        const u32 r1 = dppu<QP(0, 0, 0, 0)>(cvtpk(s0, s1));
        const u32 r2 = cvtpk(asf(r1 << 16) * 0.08838834764831845f, asf(r1 & 0xffff0000u) * 0.08838834764831845f);
        sc[2 * q] = asf(r2 << 16) * 1.4426950408889634f;
        sc[2 * q + 1] = asf(r2 & 0xffff0000u) * 1.4426950408889634f;
      }
    }
    float mw = fmaxf(fmaxf(fmaxf(sc[0], sc[1]), fmaxf(sc[2], sc[3])), fmaxf(fmaxf(sc[4], sc[5]), fmaxf(sc[6], sc[7])));
    mw = sw32max(sw16max(mw));
    float lw = 0.f;
#pragma unroll
    for (int k = 0; k < 8; ++k) { sc[k] = __builtin_amdgcn_exp2f(sc[k] - mw); lw += sc[k]; }
    lw = sw16add(lw, lw); lw = sw32add(lw, lw);
    if (l < 16 && (c & 3) == 0) { ldsf[L_ML / 4 + (c >> 2) * 8 + w] = mw; ldsf[L_ML / 4 + 32 + (c >> 2) * 8 + w] = lw; }
    STAMP(4);
    bar();
    v2u pa[8];
    {
      const v4f m0 = *(const v4f*)(ldsf + L_ML / 4 + (c >> 2) * 8), m1 = *(const v4f*)(ldsf + L_ML / 4 + (c >> 2) * 8 + 4);
      float M = fmaxf(fmaxf(fmaxf(m0.x, m0.y), fmaxf(m0.z, m0.w)), fmaxf(fmaxf(m1.x, m1.y), fmaxf(m1.z, m1.w)));
      M = asf(asu(M) & 0xffffff00u);
      if (w == 0) {
        const v4f l0 = *(const v4f*)(ldsf + L_ML / 4 + 32 + (c >> 2) * 8), l1 = *(const v4f*)(ldsf + L_ML / 4 + 32 + (c >> 2) * 8 + 4);
        float L = l0.x * __builtin_amdgcn_exp2f(m0.x - M);
        L = fmaf(l0.y, __builtin_amdgcn_exp2f(m0.y - M), L); L = fmaf(l0.z, __builtin_amdgcn_exp2f(m0.z - M), L); L = fmaf(l0.w, __builtin_amdgcn_exp2f(m0.w - M), L);
        L = fmaf(l1.x, __builtin_amdgcn_exp2f(m1.x - M), L); L = fmaf(l1.y, __builtin_amdgcn_exp2f(m1.y - M), L); L = fmaf(l1.z, __builtin_amdgcn_exp2f(m1.z - M), L); L = fmaf(l1.w, __builtin_amdgcn_exp2f(m1.w - M), L);
        const v4u u = {enc(M, T), enc(L, T), enc(M, T), enc(L, T)};
        if (l < 32 && (c & 3) == 0) st128(ws, u, (c >> 2) * 16 + g * 64, WS_ST + idx * 128);
      }
      const float cw = __builtin_amdgcn_exp2f(mw - M);
      const u32 h4 = (c & 1) ? 0xffff0000u : 0x0000ffffu;
      const u32 MA0 = (c & 2) ? 0u : h4, MA1 = (c & 2) ? h4 : 0u;
#pragma unroll
      for (int k = 0; k < 8; ++k) { const u32 d = dup(sc[k] * cw); pa[k] = v2u{d & MA0, d & MA1}; }
    }
    STAMP(5);
    {
      v4f a0 = Z4, a1 = Z4;
#pragma unroll
      for (int k = 0; k < 8; ++k) {
        const v4u v = vr[k >> 1][k & 1];
        a0 = mfma16(pa[k], v2u{v.x, v.y}, a0);
        a1 = mfma16(pa[k], v2u{v.z, v.w}, a1);
      }
      v4f* p = (v4f*)(lds + L_O + w * 2048 + g * 512 + c * 32);
      p[0] = a0; p[1] = a1;
    }
    STAMP(6);
    bar();
    {
      const float* p = ldsf + L_O / 4 + tid;
      const float s = ((p[0] + p[512]) + (p[1024] + p[1536])) + ((p[2048] + p[2560]) + (p[3072] + p[3584]));
      st32(ws, enc(s, T), tid * 4, WS_H3 + idx * 2048);
    }
    asm volatile("s_barrier" ::: "memory");
    STAMP(7);
    if (i < 14) {
      const int e = xcd * 14 + i, kq = w & 3;
      const rsrc_t rf = mk((w & 4) ? wu : wg, 512 * 1792 * 2), rd = mk(wd, 1792 * 512 * 2);
      v4u wf[4], wdn[4];
#pragma unroll
      for (int t = 0; t < 4; ++t) wf[t] = ld128<0>(rf, (l >> 1) * 3584 + (l & 1) * 16, (128 * kq + 32 * t) * 3584 + e * 32);
      if (w < 4) {
#pragma unroll
        for (int t = 0; t < 4; ++t) wdn[t] = ld128<AUX_NT>(rd, g * 1024 + c * 16, (16 * e + 4 * t) * 1024 + w * 256);
      }
      const u32 xo = ld16(rx, (4 * c + g) * 2, w * 128);
      cfence();
      float sbv, cb1;
      {
        v4u y[2];
        const u32 vo[2] = {(u32)(g * 8192 + c * 16), (u32)(g * 8192 + c * 16 + 2048)};
        gather<2>(y, ws, vo, WS_MR + 2048 + w * 256, T, false);
        sbv = rbf(tred4(asf(y[0].x), asf(y[0].y), asf(y[0].z), asf(y[0].w)));
        cb1 = rbf(1.f + rbf(tred4(asf(y[1].x), asf(y[1].y), asf(y[1].z), asf(y[1].w))));
      }
      v4u bw[4];
#pragma unroll
      for (int t = 0; t < 4; ++t) *(v4u*)(lds + L_W + w * 4096 + t * 1024 + l * 16) = wf[t];
#pragma unroll
      for (int t = 0; t < 4; ++t) {
        typedef __attribute__((address_space(3))) v4s* lp;
        const char* b = lds + L_W + w * 4096 + t * 1024 + (8 * g + (c >> 2)) * 32 + (c & 3) * 8;
        const v2u lo = __builtin_bit_cast(v2u, __builtin_amdgcn_ds_read_tr16_b64_v4i16((lp)b));
        const v2u hi = __builtin_bit_cast(v2u, __builtin_amdgcn_ds_read_tr16_b64_v4i16((lp)(b + 128)));
        bw[t] = v4u{lo.x, lo.y, hi.x, hi.y};
      }
      STAMP(8);
      float x1;
      {
        slp<DLY_H4>();
        v4u y[4];
        const u32 v0 = g * 2048 + c * 16;
        const u32 vo[4] = {v0, v0 + 8192, v0 + 16384, v0 + 24576};
        gather<4>(y, ws, vo, WS_H4 + w * 256, T, false);
        STAMP(9);
        const float s = tred4((asf(y[0].x) + asf(y[1].x)) + (asf(y[2].x) + asf(y[3].x)), (asf(y[0].y) + asf(y[1].y)) + (asf(y[2].y) + asf(y[3].y)),
                              (asf(y[0].z) + asf(y[1].z)) + (asf(y[2].z) + asf(y[3].z)), (asf(y[0].w) + asf(y[1].w)) + (asf(y[2].w) + asf(y[3].w)));
        const u32 xd = dup(asf(xo << 16) + rbf(s));
        x1 = asf(xd & 0xffff0000u);
        *(u16*)(lds + L_X1 + (64 * w + 4 * c + g) * 2) = (u16)xd;
      }
      bar();
      {
        const v4u xa = *(const v4u*)(lds + L_X1 + l * 16);
        const u32 ONE = 0x3f803f80u;
        const float s = dot2(xa.x, ONE, dot2(xa.y, ONE, 0.f)) + dot2(xa.z, ONE, dot2(xa.w, ONE, 0.f));
        const float q = dot2(xa.x, xa.x, dot2(xa.y, xa.y, 0.f)) + dot2(xa.z, xa.z, dot2(xa.w, xa.w, 0.f));
        float r = sw32add(s, q);
        r = rowsum(sw16add(r, r));
        const float mean = asf(__builtin_amdgcn_readlane(asu(r), 0)) * (1.f / 512.f);
        const float msq = asf(__builtin_amdgcn_readlane(asu(r), 32)) * (1.f / 512.f);
        const float var = rbf(fmaxf(msq - mean * mean, 0.f));
        const float den = rbf(__builtin_amdgcn_sqrtf(rbf(var + 1e-5f)));
        const float xn = rbf(rbf(x1 - rbf(mean)) * __builtin_amdgcn_rcpf(den));
        *(u16*)(lds + L_H2 + (64 * w + 4 * c + g) * 2) = (u16)dup(rbf(xn * cb1) + sbv);
      }
      bar();
      {
        v4f acc = Z4;
#pragma unroll
        for (int t = 0; t < 4; ++t) acc = mfma32(*(const v4u*)(lds + L_H2 + (128 * kq + 32 * t + 8 * g) * 2), bw[t], acc);
        if (l < 16) ldsf[L_G / 4 + (w >> 2) * 64 + c * 4 + kq] = acc.x;
      }
      STAMP(10);
      bar();
      if (w < 4) {
        const int col = 4 * (l & 3) + g;
        const v4f gp = *(const v4f*)(ldsf + L_G / 4 + col * 4), up = *(const v4f*)(ldsf + L_G / 4 + 64 + col * 4);
        const float gt = rbf((gp.x + gp.y) + (gp.z + gp.w)), uu = rbf((up.x + up.y) + (up.z + up.w));
        const float z = gt * fmaf(gt * gt, 0.044715f, 1.f) * (-2.f * 0.7978845608028654f * 1.4426950408889634f);
        const float ge = rbf(gt * __builtin_amdgcn_rcpf(1.f + __builtin_amdgcn_exp2f(z)));
        const u32 hd = dup(ge * uu);
        v4f acc = Z4;
        acc = mfma32(MK & dppu<QP(0, 0, 0, 0)>(hd), wdn[0], acc);
        acc = mfma32(MK & dppu<QP(1, 1, 1, 1)>(hd), wdn[1], acc);
        acc = mfma32(MK & dppu<QP(2, 2, 2, 2)>(hd), wdn[2], acc);
        acc = mfma32(MK & dppu<QP(3, 3, 3, 3)>(hd), wdn[3], acc);
        const v4u u = {enc(acc.x, T), enc(acc.y, T), enc(acc.z, T), enc(acc.w, T)};
        if ((g & 1) == 0) st128(ws, u, c * 32 + g * 8, WS_H5 + e * 2048 + w * 512);
      }
      STAMP(11);
    } else {
      const int r = xcd * 2 + (i - 14);
      const int col = 4 * (l & 7) + g;
      float x1 = 0.f, gb = 0.f;
      if (w == 0) {
        const u32 xo = ld16(rx, col * 2, r * 64);
        v4u y[1];
        const u32 vo[1] = {(u32)(g * 8192 + (l & 7) * 16)};
        gather<1>(y, ws, vo, WS_MR + 6144 + r * 128, T, false);
        gb = rbf(tred4(asf(y[0].x), asf(y[0].y), asf(y[0].z), asf(y[0].w)));
        v4u z[2];
        const u32 vz[2] = {(u32)((l >> 3) * 2048 + (l & 7) * 16), (u32)((l >> 3) * 2048 + (l & 7) * 16 + 16384)};
        STAMP(8);
        gather<2>(z, ws, vz, WS_H4 + r * 128, T, false);
        STAMP(9);
        float s = tred4(asf(z[0].x) + asf(z[1].x), asf(z[0].y) + asf(z[1].y), asf(z[0].z) + asf(z[1].z), asf(z[0].w) + asf(z[1].w));
        s += dppf<ROR8>(s);
        x1 = rbf(asf(xo << 16) + rbf(s));
      }
      bar();
      {
        slp<DLY_H5>();
        v4u y[2];
        const u32 v0 = (l >> 3) * 2048 + (l & 7) * 16;
        const bool two = l < 48;
        const u32 vo[2] = {v0, two ? v0 + 16384 : v0};
        gather<2>(y, ws, vo, WS_H5 + w * (14 * 2048) + r * 128, T, false);
        STAMP(10);
        const float k2 = two ? 1.f : 0.f;
        float s = tred4(fmaf(k2, asf(y[1].x), asf(y[0].x)), fmaf(k2, asf(y[1].y), asf(y[0].y)), fmaf(k2, asf(y[1].z), asf(y[0].z)), fmaf(k2, asf(y[1].w), asf(y[0].w)));
        s += dppf<ROR8>(s);
        if ((l & 8) == 0) ldsf[L_RF / 4 + w * 32 + col] = s;
      }
      bar();
      if (w == 0) {
        const float* p = ldsf + L_RF / 4 + col;
        const float s = ((p[0] + p[32]) + (p[64] + p[96])) + ((p[128] + p[160]) + (p[192] + p[224]));
        const u32 o = dup(x1 + rbf(gb * rbf(s)));
        if ((l & 8) == 0) out[r * 32 + col] = (u16)o;
        if (r == 0 && l == 0) st32(ws, T, 0, WS_EP);
      }
      STAMP(11);
    }
  }
}

extern "C" void kernel_launch(void* const* d_in, const int* in_sizes, int n_in, void* d_out, int out_size, void* d_ws, size_t ws_size,
                              hipStream_t stream) {
  (void)in_sizes; (void)n_in; (void)out_size; (void)ws_size;
  fused<<<256, 512, 0, stream>>>((const u16*)d_in[0], (const u16*)d_in[1], (const u16*)d_in[2], (const u16*)d_in[3], (const u16*)d_in[4],
                                 (const u16*)d_in[6], (const u16*)d_in[7], (const u16*)d_in[8], (const u16*)d_in[9], (const u16*)d_in[10],
                                 (u16*)d_out, (char*)d_ws);
}
```

```cpp
#define PROF 1
#define DIAG_K 0
#define DIAG_C 0
#define DIAG_M 2
#define DIAG_F 0
#include <hip/hip_runtime.h>
#include <stdint.h>

typedef unsigned int u32;
typedef unsigned short u16;
typedef u32 v4u __attribute__((ext_vector_type(4)));
typedef u32 v2u __attribute__((ext_vector_type(2)));
typedef float v4f __attribute__((ext_vector_type(4)));
typedef float v2f __attribute__((ext_vector_type(2)));
typedef __bf16 v8bf __attribute__((ext_vector_type(8)));
typedef __bf16 v2bf __attribute__((ext_vector_type(2)));
typedef short v4s __attribute__((ext_vector_type(4)));
typedef __amdgpu_buffer_rsrc_t rsrc_t;
#define DI __device__ __forceinline__

constexpr u32 WS_EP = 0;
constexpr u32 WS_H1 = 4096;
constexpr u32 WS_H2 = WS_H1 + 32768;
constexpr u32 WS_ST = WS_H2 + 65536;
constexpr u32 WS_MR = WS_ST + 16384;
constexpr u32 WS_H4 = WS_MR + 32768;
constexpr u32 WS_H3 = WS_H4 + 32768;
constexpr u32 WS_H5 = WS_H3 + 262144;
constexpr u32 WS_END = WS_H5 + 112 * 2048;
constexpr u32 WS_PROF = 1u << 20;

constexpr int AUX_NT = 2, AUX_SC1 = 16;
constexpr u32 SPIN_MAX = 1u << 16;

#define DLY_H1 0
#define DLY_H3 0
#define DLY_H4 0
#define DLY_H5 0

DI rsrc_t mk(const void* p, u32 bytes) { return __builtin_amdgcn_make_buffer_rsrc((void*)p, 0, bytes, 0x00020000); }
template <int AUX> DI v4u ld128(rsrc_t r, u32 vo, u32 so) { return __builtin_amdgcn_raw_buffer_load_b128(r, vo, so, AUX); }
template <int AUX> DI u32 ld32(rsrc_t r, u32 vo, u32 so) { return __builtin_amdgcn_raw_buffer_load_b32(r, vo, so, AUX); }
DI u32 ld16(rsrc_t r, u32 vo, u32 so) { return (u32)(u16)__builtin_amdgcn_raw_buffer_load_b16(r, vo, so, 0); }
DI void st32(rsrc_t r, u32 v, u32 vo, u32 so) { __builtin_amdgcn_raw_buffer_store_b32(v, r, vo, so, AUX_SC1); }
DI void st128(rsrc_t r, v4u v, u32 vo, u32 so) { __builtin_amdgcn_raw_buffer_store_b128(v, r, vo, so, AUX_SC1); }
DI float asf(u32 x) { return __uint_as_float(x); }
DI u32 asu(float x) { return __float_as_uint(x); }
DI u32 cvtpk(float lo, float hi) { v2f v = {lo, hi}; return __builtin_bit_cast(u32, __builtin_convertvector(v, v2bf)); }
DI u32 dup(float x) { return cvtpk(x, x); }
DI float rbf(float x) { return asf(dup(x) & 0xffff0000u); }
template <int C> DI u32 dppu(u32 x) { return (u32)__builtin_amdgcn_update_dpp(0, (int)x, C, 0xf, 0xf, true); }
template <int C> DI float dppf(float x) { return asf(dppu<C>(asu(x))); }
constexpr int ROR8 = 0x128, ROR4 = 0x124, ROR2 = 0x122, ROR1 = 0x121;
constexpr int QP(int a, int b, int c, int d) { return a | (b << 2) | (c << 4) | (d << 6); }
DI float sw32add(float a, float b) { auto r = __builtin_amdgcn_permlane32_swap(asu(a), asu(b), false, false); return asf(r[0]) + asf(r[1]); }
DI float sw16add(float a, float b) { auto r = __builtin_amdgcn_permlane16_swap(asu(a), asu(b), false, false); return asf(r[0]) + asf(r[1]); }
DI float sw32max(float a) { auto r = __builtin_amdgcn_permlane32_swap(asu(a), asu(a), false, false); return fmaxf(asf(r[0]), asf(r[1])); }
DI float sw16max(float a) { auto r = __builtin_amdgcn_permlane16_swap(asu(a), asu(a), false, false); return fmaxf(asf(r[0]), asf(r[1])); }
DI float tred4(float x, float y, float z, float w) { return sw16add(sw32add(x, z), sw32add(y, w)); }
DI float rowsum(float v) { v += dppf<ROR8>(v); v += dppf<ROR4>(v); v += dppf<ROR2>(v); v += dppf<ROR1>(v); return v; }
DI float rowmax(float v) { v = fmaxf(v, dppf<ROR8>(v)); v = fmaxf(v, dppf<ROR4>(v)); v = fmaxf(v, dppf<ROR2>(v)); v = fmaxf(v, dppf<ROR1>(v)); return v; }
DI float wavesum(float v) { v = rowsum(v); v = sw16add(v, v); return sw32add(v, v); }
DI u32 enc(float v, u32 T) { return (asu(v) & 0xffffff00u) | T; }
DI void cfence() { asm volatile("" ::: "memory"); }
DI void bar() { asm volatile("s_waitcnt lgkmcnt(0)\n\ts_barrier" ::: "memory"); }
DI v4f mfma32(v4u a, v4u b, v4f c) { return __builtin_amdgcn_mfma_f32_16x16x32_bf16(__builtin_bit_cast(v8bf, a), __builtin_bit_cast(v8bf, b), c, 0, 0, 0); }
DI v4f mfma16(v2u a, v2u b, v4f c) { return __builtin_amdgcn_mfma_f32_16x16x16bf16_1k(__builtin_bit_cast(v4s, a), __builtin_bit_cast(v4s, b), c, 0, 0, 0); }
DI float silu_bf(u32 h) { float v = asf(h << 16); return v * __builtin_amdgcn_rcpf(1.f + __builtin_amdgcn_exp2f(-1.4426950408889634f * v)); }
DI float dot2(u32 a, u32 b, float c) { return __builtin_amdgcn_fdot2_f32_bf16(__builtin_bit_cast(v2bf, a), __builtin_bit_cast(v2bf, b), c, false); }
template <int N> DI void slp() { if (N > 0) __builtin_amdgcn_s_sleep(N); }

DI u32 untag(v4u& y, u32 T) { y.x ^= T; y.y ^= T; y.z ^= T; y.w ^= T; return y.x | y.y | y.z | y.w; }
template <int N> DI void gather(v4u (&y)[N], rsrc_t ws, const u32 (&vo)[N], u32 so, u32 T, bool have) {
  if (__builtin_amdgcn_ballot_w64(!have) == 0) return;
#pragma unroll 1
  for (u32 spin = 0; spin < SPIN_MAX; ++spin) {
    cfence();
#pragma unroll
    for (int k = 0; k < N; ++k) y[k] = ld128<AUX_SC1>(ws, vo[k], so);
    u32 bad = 0;
#pragma unroll
    for (int k = 0; k < N; ++k) bad |= untag(y[k], T);
    if (__builtin_amdgcn_ballot_w64((bad & 255u) != 0) == 0) break;
  }
}

#define STAMP(k) do { ts[k] = (u32)__builtin_amdgcn_s_memrealtime(); } while (0)

constexpr u32 L_RA = 0, L_RB = 2048, L_RM = 6144, L_RD = 8192;
constexpr u32 L_Q = 0, L_ML = 1024, L_X1 = 1536, L_H2 = 2560, L_G = 3584, L_RF = 4096, L_O = 8192, L_W = 24576;
constexpr u32 LDS_BYTES = 84 * 1024;

__global__ void __launch_bounds__(512, 2)
fused(const u16* __restrict__ xin, const u16* __restrict__ kc, const u16* __restrict__ vc, const u16* __restrict__ wq,
      const u16* __restrict__ wo, const u16* __restrict__ wg, const u16* __restrict__ wu, const u16* __restrict__ wd,
      const u16* __restrict__ cin, const u16* __restrict__ wmod, u16* __restrict__ out, char* wsp) {
  __shared__ __attribute__((aligned(16))) char lds[LDS_BYTES];
  u32 ts[16];
#pragma unroll
  for (int k = 0; k < 16; ++k) ts[k] = 0;
  STAMP(0);
  const int tid = threadIdx.x;
  const int w = __builtin_amdgcn_readfirstlane(tid >> 6);
  const int l = tid & 63, g = l >> 4, c = l & 15;
  const int xcd = blockIdx.x & 7, role = (blockIdx.x >> 3) & 1, i = blockIdx.x >> 4;
  const int idx = xcd * 16 + i;
  const rsrc_t ws = mk(wsp, WS_END);
  const rsrc_t rx = mk(xin, 1024);
  const u32 ep = ld32<AUX_SC1>(ws, 0, WS_EP);
  const int jj = (c & 3) + 4 * (c >> 3);
  const u32 hm = (jj & 1) ? 0xffff0000u : 0x0000ffffu;
  const v4u MK = {(jj >> 1) == 0 ? hm : 0u, (jj >> 1) == 1 ? hm : 0u, (jj >> 1) == 2 ? hm : 0u, (jj >> 1) == 3 ? hm : 0u};
  const v4f Z4 = {0.f, 0.f, 0.f, 0.f};
  float* ldsf = (float*)lds;

  if (role == 0) {
    const rsrc_t rmod = mk(wmod, 512 * 3072 * 2), rc = mk(cin, 1024), rwq = mk(wq, 512 * 4096 * 2), rwo = mk(wo, 4096 * 512 * 2);
    const int ka = idx >> 4, atc = idx & 15, kb = idx >> 5, head = idx & 31;
    const int a = (l >> 2) & 1;
    const u32 rowsel = 4 * (c >> 3) + g;
    const u32 cA = ld16(rc, (4 * a + g) * 2, (64 * ka + 8 * w) * 2);
    const v4u wA = ld128<AUX_NT>(rmod, rowsel * 6144 + (c & 7) * 16, (64 * ka + 8 * w) * 6144 + atc * 128);
    const v4u xv = ld128<0>(rx, l * 16, 0);
    const u32 xo = ld16(rx, (4 * (l & 3) + g) * 2, (128 * kb + 16 * w) * 2);
    const u32 cM0 = ld16(rc, (4 * a + g) * 2, (128 * kb + 16 * w) * 2);
    const u32 cM1 = ld16(rc, (4 * a + g) * 2 + 16, (128 * kb + 16 * w) * 2);
    cfence();
    v4u wB[4];
#pragma unroll
    for (int t = 0; t < 4; ++t) wB[t] = ld128<AUX_NT>(rwq, g * 8192 + c * 16, (128 * kb + 16 * w + 4 * t) * 8192 + head * 256);
    cfence();
    const u32 T = ((u32)__builtin_amdgcn_readfirstlane(ep) + 1u) & 255u;
    {
      const u32 sd = dup(silu_bf(cA));
      const v4f acc = mfma32(MK & sd, wA, Z4);
      auto p0 = __builtin_amdgcn_permlane16_swap(asu(acc.x), asu(acc.y), false, false);
      auto p1 = __builtin_amdgcn_permlane16_swap(asu(acc.z), asu(acc.w), false, false);
      const float s0 = asf(p0[0]) + dppf<ROR8>(asf(p0[1])), s1 = asf(p1[0]) + dppf<ROR8>(asf(p1[1]));
      if (c < 8) { float* p = ldsf + L_RA / 4 + w * 64 + 8 * c + (g & 1) + 4 * (g >> 1); p[0] = s0; p[2] = s1; }
    }
    STAMP(1);
    bar();
    if (w == 0) {
      const float* p = ldsf + L_RA / 4 + l;
      const float s = ((p[0] + p[64]) + (p[128] + p[192])) + ((p[256] + p[320]) + (p[384] + p[448]));
      st32(ws, enc(s, T), l * 4, WS_H1 + (ka * 16 + atc) * 256);
    }
    STAMP(2);
    float xn;
    {
      const u32 ONE = 0x3f803f80u;
      float s = dot2(xv.x, ONE, dot2(xv.y, ONE, 0.f)) + dot2(xv.z, ONE, dot2(xv.w, ONE, 0.f));
      const float mean = wavesum(s) * (1.f / 512.f);
      float q = 0.f;
      const u32 xs[4] = {xv.x, xv.y, xv.z, xv.w};
#pragma unroll
      for (int k = 0; k < 4; ++k) { const float d0 = asf(xs[k] << 16) - mean, d1 = asf(xs[k] & 0xffff0000u) - mean; q = fmaf(d0, d0, fmaf(d1, d1, q)); }
      const float var = rbf(wavesum(q) * (1.f / 512.f));
      const float den = rbf(__builtin_amdgcn_sqrtf(rbf(var + 1e-5f)));
      xn = rbf(rbf(asf(xo << 16) - rbf(mean)) * __builtin_amdgcn_rcpf(den));
    }
    const u32 sM0 = dup(silu_bf(cM0)), sM1 = dup(silu_bf(cM1));
    {
      slp<DLY_H1>();
      v4u y[1];
      const u32 vo[1] = {(u32)((l >> 2) * 2048 + (l & 3) * 16)};
      gather<1>(y, ws, vo, WS_H1 + (128 * kb + 16 * w) * 4, T, false);
      STAMP(3);
      float v = tred4(asf(y[0].x), asf(y[0].y), asf(y[0].z), asf(y[0].w));
      v = rbf(v + dppf<ROR8>(v));
      const float o = dppf<ROR4>(v);
      const float sa = a ? o : v, ca = a ? v : o;
      const u32 hd = dup(rbf(xn * rbf(1.f + ca)) + sa);
      v4f acc = Z4;
      acc = mfma32(MK & dppu<QP(0, 0, 0, 0)>(hd), wB[0], acc);
      acc = mfma32(MK & dppu<QP(1, 1, 1, 1)>(hd), wB[1], acc);
      acc = mfma32(MK & dppu<QP(2, 2, 2, 2)>(hd), wB[2], acc);
      acc = mfma32(MK & dppu<QP(3, 3, 3, 3)>(hd), wB[3], acc);
      if ((g & 1) == 0) *(v4f*)(ldsf + L_RB / 4 + w * 128 + 8 * c + 2 * g) = acc;
    }
    STAMP(4);
    bar();
    if (w < 2) {
      const float* p = ldsf + L_RB / 4 + tid;
      const float s = ((p[0] + p[128]) + (p[256] + p[384])) + ((p[512] + p[640]) + (p[768] + p[896]));
      st32(ws, enc(s, T), tid * 4, WS_H2 + kb * 16384 + head * 512);
    }
    asm volatile("s_barrier" ::: "memory");
    STAMP(5);
    const int I = idx >> 3, J = idx & 7;
    v4u wM[2], wD[4];
#pragma unroll
    for (int t = 0; t < 2; ++t) wM[t] = ld128<AUX_NT>(rmod, rowsel * 6144 + (c & 7) * 16, (128 * kb + 16 * w + 8 * t) * 6144 + 2048 + head * 128);
#pragma unroll
    for (int t = 0; t < 4; ++t) wD[t] = ld128<AUX_NT>(rwo, (16 * (c >> 3) + g) * 1024 + (c & 7) * 16, (256 * I + 32 * w + 4 * t) * 1024 + J * 128);
    cfence();
    u32 pf;
    {
      const rsrc_t rf = mk((w & 4) ? wu : wg, 512 * 1792 * 2);
      pf = ld32<0>(rf, (l >> 2) * 3584 + (l & 3) * 128, (i * 32 + (w & 1) * 16) * 3584 + ((xcd * 448) & ~127));
      if (w & 2) pf = 0;
    }
    cfence();
    {
      v4f acc = mfma32(MK & sM0, wM[0], Z4);
      acc = mfma32(MK & sM1, wM[1], acc);
      auto p0 = __builtin_amdgcn_permlane16_swap(asu(acc.x), asu(acc.y), false, false);
      auto p1 = __builtin_amdgcn_permlane16_swap(asu(acc.z), asu(acc.w), false, false);
      const float s0 = asf(p0[0]) + dppf<ROR8>(asf(p0[1])), s1 = asf(p1[0]) + dppf<ROR8>(asf(p1[1]));
      if (c < 8) { float* p = ldsf + L_RM / 4 + w * 64 + 8 * c + (g & 1) + 4 * (g >> 1); p[0] = s0; p[2] = s1; }
    }
    bar();
    if (w == 1) {
      const float* p = ldsf + L_RM / 4 + l;
      const float s = ((p[0] + p[64]) + (p[128] + p[192])) + ((p[256] + p[320]) + (p[384] + p[448]));
      st32(ws, enc(s, T), l * 4, WS_MR + kb * 8192 + head * 256);
    }
    STAMP(6);
    const int gk = I >> 1, hl = 2 * (I & 1) + (w >> 2);
    float f0, f1;
    {
      v4u y[1];
      const u32 vo[1] = {(u32)(c * 128)};
      gather<1>(y, ws, vo, WS_ST + gk * 2048 + hl * 16, T, false);
      const float m = asf(y[0].x), M = rowmax(m);
      const float e = __builtin_amdgcn_exp2f(m - M);
      const float f = e * __builtin_amdgcn_rcpf(rowsum(e * asf(y[0].y)));
      f0 = asf(__builtin_amdgcn_ds_bpermute((l >> 3) * 4, asu(f)));
      f1 = asf(__builtin_amdgcn_ds_bpermute((l >> 3) * 4 + 32, asu(f)));
    }
    STAMP(7);
    float ga = 0.f;
    if (w == 0) {
      v4u y[1];
      const u32 vo[1] = {(u32)(g * 8192 + c * 16)};
      gather<1>(y, ws, vo, WS_MR + J * 256, T, false);
      ga = rbf(tred4(asf(y[0].x), asf(y[0].y), asf(y[0].z), asf(y[0].w)));
    }
    STAMP(8);
    {
      slp<DLY_H3>();
      v4u y[2];
      const u32 vo[2] = {(u32)((l >> 3) * 2048 + (l & 7) * 16), (u32)((l >> 3) * 2048 + (l & 7) * 16 + 16384)};
      gather<2>(y, ws, vo, WS_H3 + gk * 32768 + hl * 512 + (w & 3) * 128, T, false);
      STAMP(9);
      float v = tred4(fmaf(f0, asf(y[0].x), f1 * asf(y[1].x)), fmaf(f0, asf(y[0].y), f1 * asf(y[1].y)),
                      fmaf(f0, asf(y[0].z), f1 * asf(y[1].z)), fmaf(f0, asf(y[0].w), f1 * asf(y[1].w)));
      const u32 od = dup(v + dppf<ROR8>(v));
      v4f acc = Z4;
      acc = mfma32(MK & dppu<QP(0, 0, 0, 0)>(od), wD[0], acc);
      acc = mfma32(MK & dppu<QP(1, 1, 1, 1)>(od), wD[1], acc);
      acc = mfma32(MK & dppu<QP(2, 2, 2, 2)>(od), wD[2], acc);
      acc = mfma32(MK & dppu<QP(3, 3, 3, 3)>(od), wD[3], acc);
      auto p0 = __builtin_amdgcn_permlane16_swap(asu(acc.x), asu(acc.y), false, false);
      auto p1 = __builtin_amdgcn_permlane16_swap(asu(acc.z), asu(acc.w), false, false);
      const float s0 = asf(p0[0]) + dppf<ROR8>(asf(p0[1])), s1 = asf(p1[0]) + dppf<ROR8>(asf(p1[1]));
      if (c < 8) { float* p = ldsf + L_RD / 4 + w * 64 + 8 * c + (g & 1) + 4 * (g >> 1); p[0] = s0; p[2] = s1; }
    }
    bar();
    if (w == 0) {
      const float* p = ldsf + L_RD / 4 + 4 * c + g;
      const float s = ((p[0] + p[64]) + (p[128] + p[192])) + ((p[256] + p[320]) + (p[384] + p[448]));
      st32(ws, enc(s * ga, T), (4 * c + g) * 4, WS_H4 + I * 2048 + J * 256);
    }
    STAMP(10);
    asm volatile("" :: "v"(pf));
  } else {
    const rsrc_t rk = mk(kc + ((size_t)xcd * 65536 + 61440) * 128, 4096 * 256), rv = mk(vc + ((size_t)xcd * 65536 + 61440) * 128, 4096 * 256);
    const int rot = w + i + xcd;
    const u32 voK = (c & 7) * 256 + (2 * g + (c >> 3)) * 16, voV = (4 * (g & 1) + (g >> 1)) * 256 + c * 16;
    const u32 sb0 = i * 65536 + w * 8192;
    v4u kr[4][2], vr[4][2];
#pragma unroll
    for (int q = 0; q < 4; ++q) {
      kr[q][0] = ld128<AUX_NT>(rk, voK, sb0 + ((q + rot) & 3) * 2048);
      kr[q][1] = ld128<AUX_NT>(rk, voK + 128, sb0 + ((q + rot) & 3) * 2048);
    }
    cfence();
#pragma unroll
    for (int q = 0; q < 2; ++q) {
      vr[q][0] = ld128<AUX_NT>(rv, voV, sb0 + ((q + rot) & 3) * 2048);
      vr[q][1] = ld128<AUX_NT>(rv, voV + 512, sb0 + ((q + rot) & 3) * 2048);
    }
    asm volatile("s_waitcnt vmcnt(4)" ::: "memory");
    STAMP(1);
    const u32 T = ((u32)__builtin_amdgcn_readfirstlane(ep) + 1u) & 255u;
    {
      v4u y[1];
      const u32 vo[1] = {(u32)(g * 16384 + c * 16)};
      y[0] = ld128<AUX_SC1>(ws, vo[0], WS_H2 + xcd * 2048 + w * 256);
      cfence();
#pragma unroll
      for (int q = 2; q < 4; ++q) {
        vr[q][0] = ld128<AUX_NT>(rv, voV, sb0 + ((q + rot) & 3) * 2048);
        vr[q][1] = ld128<AUX_NT>(rv, voV + 512, sb0 + ((q + rot) & 3) * 2048);
      }
      cfence();
      const bool ok = (untag(y[0], T) & 255u) == 0;
      STAMP(2);
      gather<1>(y, ws, vo, WS_H2 + xcd * 2048 + w * 256, T, ok);
      STAMP(3);
      const float qv = tred4(asf(y[0].x), asf(y[0].y), asf(y[0].z), asf(y[0].w));
      *(u16*)(lds + L_Q + (64 * w + 4 * c + g) * 2) = (u16)dup(qv);
    }
    bar();
    float sc[8];
    {
      const v4u q0 = *(const v4u*)(lds + L_Q + (c >> 2) * 256 + (2 * g + (c & 1)) * 16);
      const v4u q1 = *(const v4u*)(lds + L_Q + (c >> 2) * 256 + (2 * g + (c & 1)) * 16 + 128);
#pragma unroll
      for (int q = 0; q < 4; ++q) {
        const v4f d = mfma32(kr[q][1], q1, mfma32(kr[q][0], q0, Z4));
        auto p0 = __builtin_amdgcn_permlane32_swap(asu(d.x), asu(d.y), false, false);
        auto p1 = __builtin_amdgcn_permlane32_swap(asu(d.z), asu(d.w), false, false);
        const float s0 = asf(p0[0]) + dppf<QP(1, 1, 1, 1)>(asf(p0[1])), s1 = asf(p1[0]) + dppf<QP(1, 1, 1, 1)>(asf(p1[1]));
        const u32 r1 = dppu<QP(0, 0, 0, 0)>(cvtpk(s0, s1));
        const u32 r2 = cvtpk(asf(r1 << 16) * 0.08838834764831845f, asf(r1 & 0xffff0000u) * 0.08838834764831845f);
        sc[2 * q] = asf(r2 << 16) * 1.4426950408889634f;
        sc[2 * q + 1] = asf(r2 & 0xffff0000u) * 1.4426950408889634f;
      }
    }
    float mw = fmaxf(fmaxf(fmaxf(sc[0], sc[1]), fmaxf(sc[2], sc[3])), fmaxf(fmaxf(sc[4], sc[5]), fmaxf(sc[6], sc[7])));
    mw = sw32max(sw16max(mw));
    float lw = 0.f;
#pragma unroll
    for (int k = 0; k < 8; ++k) { sc[k] = __builtin_amdgcn_exp2f(sc[k] - mw); lw += sc[k]; }
    lw = sw16add(lw, lw); lw = sw32add(lw, lw);
    if (l < 16 && (c & 3) == 0) { ldsf[L_ML / 4 + (c >> 2) * 8 + w] = mw; ldsf[L_ML / 4 + 32 + (c >> 2) * 8 + w] = lw; }
    STAMP(4);
    bar();
    v2u pa[8];
    {
      const v4f m0 = *(const v4f*)(ldsf + L_ML / 4 + (c >> 2) * 8), m1 = *(const v4f*)(ldsf + L_ML / 4 + (c >> 2) * 8 + 4);
      float M = fmaxf(fmaxf(fmaxf(m0.x, m0.y), fmaxf(m0.z, m0.w)), fmaxf(fmaxf(m1.x, m1.y), fmaxf(m1.z, m1.w)));
      M = asf(asu(M) & 0xffffff00u);
      if (w == 0) {
        const v4f l0 = *(const v4f*)(ldsf + L_ML / 4 + 32 + (c >> 2) * 8), l1 = *(const v4f*)(ldsf + L_ML / 4 + 32 + (c >> 2) * 8 + 4);
        float L = l0.x * __builtin_amdgcn_exp2f(m0.x - M);
        L = fmaf(l0.y, __builtin_amdgcn_exp2f(m0.y - M), L); L = fmaf(l0.z, __builtin_amdgcn_exp2f(m0.z - M), L); L = fmaf(l0.w, __builtin_amdgcn_exp2f(m0.w - M), L);
        L = fmaf(l1.x, __builtin_amdgcn_exp2f(m1.x - M), L); L = fmaf(l1.y, __builtin_amdgcn_exp2f(m1.y - M), L); L = fmaf(l1.z, __builtin_amdgcn_exp2f(m1.z - M), L); L = fmaf(l1.w, __builtin_amdgcn_exp2f(m1.w - M), L);
        const v4u u = {enc(M, T), enc(L, T), enc(M, T), enc(L, T)};
        if (l < 32 && (c & 3) == 0) st128(ws, u, (c >> 2) * 16 + g * 64, WS_ST + idx * 128);
      }
      const float cw = __builtin_amdgcn_exp2f(mw - M);
      const u32 h4 = (c & 1) ? 0xffff0000u : 0x0000ffffu;
      const u32 MA0 = (c & 2) ? 0u : h4, MA1 = (c & 2) ? h4 : 0u;
#pragma unroll
      for (int k = 0; k < 8; ++k) { const u32 d = dup(sc[k] * cw); pa[k] = v2u{d & MA0, d & MA1}; }
    }
    STAMP(5);
    {
      v4f a0 = Z4, a1 = Z4;
#pragma unroll
      for (int k = 0; k < 8; ++k) {
        const v4u v = vr[k >> 1][k & 1];
        a0 = mfma16(pa[k], v2u{v.x, v.y}, a0);
        a1 = mfma16(pa[k], v2u{v.z, v.w}, a1);
      }
      v4f* p = (v4f*)(lds + L_O + w * 2048 + g * 512 + c * 32);
      p[0] = a0; p[1] = a1;
    }
    STAMP(6);
    bar();
    {
      const float* p = ldsf + L_O / 4 + tid;
      const float s = ((p[0] + p[512]) + (p[1024] + p[1536])) + ((p[2048] + p[2560]) + (p[3072] + p[3584]));
      st32(ws, enc(s, T), tid * 4, WS_H3 + idx * 2048);
    }
    asm volatile("s_barrier" ::: "memory");
    STAMP(7);
    if (i < 14) {
      const int e = xcd * 14 + i, kq = w & 3;
      const rsrc_t rf = mk((w & 4) ? wu : wg, 512 * 1792 * 2), rd = mk(wd, 1792 * 512 * 2);
      v4u wf[4], wdn[4];
#pragma unroll
      for (int t = 0; t < 4; ++t) wf[t] = ld128<0>(rf, (l >> 1) * 3584 + (l & 1) * 16, (128 * kq + 32 * t) * 3584 + e * 32);
      if (w < 4) {
#pragma unroll
        for (int t = 0; t < 4; ++t) wdn[t] = ld128<AUX_NT>(rd, g * 1024 + c * 16, (16 * e + 4 * t) * 1024 + w * 256);
      }
      const u32 xo = ld16(rx, (4 * c + g) * 2, w * 128);
      cfence();
      float sbv, cb1;
      {
        v4u y[2];
        const u32 vo[2] = {(u32)(g * 8192 + c * 16), (u32)(g * 8192 + c * 16 + 2048)};
        gather<2>(y, ws, vo, WS_MR + 2048 + w * 256, T, false);
        sbv = rbf(tred4(asf(y[0].x), asf(y[0].y), asf(y[0].z), asf(y[0].w)));
        cb1 = rbf(1.f + rbf(tred4(asf(y[1].x), asf(y[1].y), asf(y[1].z), asf(y[1].w))));
      }
      v4u bw[4];
#pragma unroll
      for (int t = 0; t < 4; ++t) *(v4u*)(lds + L_W + w * 4096 + t * 1024 + l * 16) = wf[t];
#pragma unroll
      for (int t = 0; t < 4; ++t) {
        typedef __attribute__((address_space(3))) v4s* lp;
        const char* b = lds + L_W + w * 4096 + t * 1024 + (8 * g + (c >> 2)) * 32 + (c & 3) * 8;
        const v2u lo = __builtin_bit_cast(v2u, __builtin_amdgcn_ds_read_tr16_b64_v4i16((lp)b));
        const v2u hi = __builtin_bit_cast(v2u, __builtin_amdgcn_ds_read_tr16_b64_v4i16((lp)(b + 128)));
        bw[t] = v4u{lo.x, lo.y, hi.x, hi.y};
      }
      STAMP(8);
      float x1;
      {
        slp<DLY_H4>();
        v4u y[4];
        const u32 v0 = g * 2048 + c * 16;
        const u32 vo[4] = {v0, v0 + 8192, v0 + 16384, v0 + 24576};
        gather<4>(y, ws, vo, WS_H4 + w * 256, T, false);
        STAMP(9);
        const float s = tred4((asf(y[0].x) + asf(y[1].x)) + (asf(y[2].x) + asf(y[3].x)), (asf(y[0].y) + asf(y[1].y)) + (asf(y[2].y) + asf(y[3].y)),
                              (asf(y[0].z) + asf(y[1].z)) + (asf(y[2].z) + asf(y[3].z)), (asf(y[0].w) + asf(y[1].w)) + (asf(y[2].w) + asf(y[3].w)));
        const u32 xd = dup(asf(xo << 16) + rbf(s));
        x1 = asf(xd & 0xffff0000u);
        *(u16*)(lds + L_X1 + (64 * w + 4 * c + g) * 2) = (u16)xd;
      }
      bar();
      {
        const v4u xa = *(const v4u*)(lds + L_X1 + l * 16);
        const u32 ONE = 0x3f803f80u;
        const float s = dot2(xa.x, ONE, dot2(xa.y, ONE, 0.f)) + dot2(xa.z, ONE, dot2(xa.w, ONE, 0.f));
        const float q = dot2(xa.x, xa.x, dot2(xa.y, xa.y, 0.f)) + dot2(xa.z, xa.z, dot2(xa.w, xa.w, 0.f));
        float r = sw32add(s, q);
        r = rowsum(sw16add(r, r));
        const float mean = asf(__builtin_amdgcn_readlane(asu(r), 0)) * (1.f / 512.f);
        const float msq = asf(__builtin_amdgcn_readlane(asu(r), 32)) * (1.f / 512.f);
        const float var = rbf(fmaxf(msq - mean * mean, 0.f));
        const float den = rbf(__builtin_amdgcn_sqrtf(rbf(var + 1e-5f)));
        const float xn = rbf(rbf(x1 - rbf(mean)) * __builtin_amdgcn_rcpf(den));
        *(u16*)(lds + L_H2 + (64 * w + 4 * c + g) * 2) = (u16)dup(rbf(xn * cb1) + sbv);
      }
      bar();
      {
        v4f acc = Z4;
#pragma unroll
        for (int t = 0; t < 4; ++t) acc = mfma32(*(const v4u*)(lds + L_H2 + (128 * kq + 32 * t + 8 * g) * 2), bw[t], acc);
        if (l < 16) ldsf[L_G / 4 + (w >> 2) * 64 + c * 4 + kq] = acc.x;
      }
      STAMP(10);
      bar();
      if (w < 4) {
        const int col = 4 * (l & 3) + g;
        const v4f gp = *(const v4f*)(ldsf + L_G / 4 + col * 4), up = *(const v4f*)(ldsf + L_G / 4 + 64 + col * 4);
        const float gt = rbf((gp.x + gp.y) + (gp.z + gp.w)), uu = rbf((up.x + up.y) + (up.z + up.w));
        const float z = gt * fmaf(gt * gt, 0.044715f, 1.f) * (-2.f * 0.7978845608028654f * 1.4426950408889634f);
        const float ge = rbf(gt * __builtin_amdgcn_rcpf(1.f + __builtin_amdgcn_exp2f(z)));
        const u32 hd = dup(ge * uu);
        v4f acc = Z4;
        acc = mfma32(MK & dppu<QP(0, 0, 0, 0)>(hd), wdn[0], acc);
        acc = mfma32(MK & dppu<QP(1, 1, 1, 1)>(hd), wdn[1], acc);
        acc = mfma32(MK & dppu<QP(2, 2, 2, 2)>(hd), wdn[2], acc);
        acc = mfma32(MK & dppu<QP(3, 3, 3, 3)>(hd), wdn[3], acc);
        const v4u u = {enc(acc.x, T), enc(acc.y, T), enc(acc.z, T), enc(acc.w, T)};
        if ((g & 1) == 0) st128(ws, u, c * 32 + g * 8, WS_H5 + e * 2048 + w * 512);
      }
      STAMP(11);
    } else {
      const int r = xcd * 2 + (i - 14);
      const int col = 4 * (l & 7) + g;
      float x1 = 0.f, gb = 0.f;
      if (w == 0) {
        const u32 xo = ld16(rx, col * 2, r * 64);
        v4u y[1];
        const u32 vo[1] = {(u32)(g * 8192 + (l & 7) * 16)};
        gather<1>(y, ws, vo, WS_MR + 6144 + r * 128, T, false);
        gb = rbf(tred4(asf(y[0].x), asf(y[0].y), asf(y[0].z), asf(y[0].w)));
        v4u z[2];
        const u32 vz[2] = {(u32)((l >> 3) * 2048 + (l & 7) * 16), (u32)((l >> 3) * 2048 + (l & 7) * 16 + 16384)};
        STAMP(8);
        gather<2>(z, ws, vz, WS_H4 + r * 128, T, false);
        STAMP(9);
        float s = tred4(asf(z[0].x) + asf(z[1].x), asf(z[0].y) + asf(z[1].y), asf(z[0].z) + asf(z[1].z), asf(z[0].w) + asf(z[1].w));
        s += dppf<ROR8>(s);
        x1 = rbf(asf(xo << 16) + rbf(s));
      }
      bar();
      {
        slp<DLY_H5>();
        v4u y[2];
        const u32 v0 = (l >> 3) * 2048 + (l & 7) * 16;
        const bool two = l < 48;
        const u32 vo[2] = {v0, two ? v0 + 16384 : v0};
        gather<2>(y, ws, vo, WS_H5 + w * (14 * 2048) + r * 128, T, false);
        STAMP(10);
        const float k2 = two ? 1.f : 0.f;
        float s = tred4(fmaf(k2, asf(y[1].x), asf(y[0].x)), fmaf(k2, asf(y[1].y), asf(y[0].y)), fmaf(k2, asf(y[1].z), asf(y[0].z)), fmaf(k2, asf(y[1].w), asf(y[0].w)));
        s += dppf<ROR8>(s);
        if ((l & 8) == 0) ldsf[L_RF / 4 + w * 32 + col] = s;
      }
      bar();
      if (w == 0) {
        const float* p = ldsf + L_RF / 4 + col;
        const float s = ((p[0] + p[32]) + (p[64] + p[96])) + ((p[128] + p[160]) + (p[192] + p[224]));
        const u32 o = dup(x1 + rbf(gb * rbf(s)));
        if ((l & 8) == 0) out[r * 32 + col] = (u16)o;
        if (r == 0 && l == 0) st32(ws, T, 0, WS_EP);
      }
      STAMP(11);
    }
  }
  {
    const rsrc_t rp = mk(wsp + WS_PROF, 2048 * 64);
    if (l == 0) {
#pragma unroll
      for (int k = 0; k < 4; ++k) st128(rp, v4u{ts[4 * k], ts[4 * k + 1], ts[4 * k + 2], ts[4 * k + 3]}, k * 16, (blockIdx.x * 8 + w) * 64);
    }
    if (blockIdx.x == 8 + 14 * 16) {
      const u32 tend = (u32)__builtin_amdgcn_s_memrealtime();
      while ((u32)__builtin_amdgcn_s_memrealtime() - tend < 300u) __builtin_amdgcn_s_sleep(2);
      u32* sh = (u32*)lds;
      bar();
      if (tid == 0) { sh[0] = 0; sh[1] = 0; sh[2] = 0xffffffffu; sh[3] = 0; sh[4] = 0; }
      bar();
      for (int k = 0; k < 4; ++k) {
        const int wv = tid * 4 + k, blk = wv >> 3;
        const int cl = ((blk >> 3) & 1) == 0 ? 0 : ((blk >> 4) < 14 ? 1 : 2);
        const u32 a0 = tend - ld32<AUX_SC1>(rp, wv * 64, 0), ak = tend - ld32<AUX_SC1>(rp, wv * 64 + DIAG_K * 4, 0);
        if (a0 < 6000u) atomicMax(sh + 0, a0);
        if (cl == DIAG_C && (int)ak > -400 && (int)ak < 6000) { const u32 b = ak + 1000u; atomicMax(sh + 1, b); atomicMin(sh + 2, b); atomicAdd(sh + 3, b); atomicAdd(sh + 4, 1u); }
      }
      bar();
      const u32 first = sh[0] + 1000u;
      const u32 q = DIAG_M == 0 ? first - sh[1] : DIAG_M == 1 ? first - sh[3] / (sh[4] ? sh[4] : 1u) : first - sh[2];
      const u32 t1 = (u32)__builtin_amdgcn_s_memrealtime();
      const u32 dl = min(q, 6000u) * DIAG_F;
      while ((u32)__builtin_amdgcn_s_memrealtime() - t1 < dl) __builtin_amdgcn_s_sleep(2);
    }
  }
}

extern "C" void kernel_launch(void* const* d_in, const int* in_sizes, int n_in, void* d_out, int out_size, void* d_ws, size_t ws_size,
                              hipStream_t stream) {
  (void)in_sizes; (void)n_in; (void)out_size; (void)ws_size;
  fused<<<256, 512, 0, stream>>>((const u16*)d_in[0], (const u16*)d_in[1], (const u16*)d_in[2], (const u16*)d_in[3], (const u16*)d_in[4],
                                 (const u16*)d_in[6], (const u16*)d_in[7], (const u16*)d_in[8], (const u16*)d_in[9], (const u16*)d_in[10],
                                 (u16*)d_out, (char*)d_ws);
}
```

```cpp
#define PADALL 0
#define PADD 4000u
#include <hip/hip_runtime.h>
#include <stdint.h>

typedef unsigned int u32;
typedef unsigned short u16;
typedef u32 v4u __attribute__((ext_vector_type(4)));
typedef u32 v2u __attribute__((ext_vector_type(2)));
typedef float v4f __attribute__((ext_vector_type(4)));
typedef float v2f __attribute__((ext_vector_type(2)));
typedef __bf16 v8bf __attribute__((ext_vector_type(8)));
typedef __bf16 v2bf __attribute__((ext_vector_type(2)));
typedef short v4s __attribute__((ext_vector_type(4)));
typedef __amdgpu_buffer_rsrc_t rsrc_t;
#define DI __device__ __forceinline__

#define PROF 0

constexpr u32 WS_EP = 0;
constexpr u32 WS_H1 = 4096;
constexpr u32 WS_H2 = WS_H1 + 32768;
constexpr u32 WS_ST = WS_H2 + 65536;
constexpr u32 WS_MR = WS_ST + 16384;
constexpr u32 WS_H4 = WS_MR + 32768;
constexpr u32 WS_H3 = WS_H4 + 32768;
constexpr u32 WS_H5 = WS_H3 + 262144;
constexpr u32 WS_END = WS_H5 + 112 * 2048;
constexpr u32 WS_PROF = 1u << 20;

constexpr int AUX_NT = 2, AUX_SC1 = 16;
constexpr u32 SPIN_MAX = 1u << 16;

#define DLY_H1 0
#define DLY_H3 0
#define DLY_H4 0
#define DLY_H5 0

DI rsrc_t mk(const void* p, u32 bytes) { return __builtin_amdgcn_make_buffer_rsrc((void*)p, 0, bytes, 0x00020000); }
template <int AUX> DI v4u ld128(rsrc_t r, u32 vo, u32 so) { return __builtin_amdgcn_raw_buffer_load_b128(r, vo, so, AUX); }
template <int AUX> DI u32 ld32(rsrc_t r, u32 vo, u32 so) { return __builtin_amdgcn_raw_buffer_load_b32(r, vo, so, AUX); }
DI u32 ld16(rsrc_t r, u32 vo, u32 so) { return (u32)(u16)__builtin_amdgcn_raw_buffer_load_b16(r, vo, so, 0); }
DI void st32(rsrc_t r, u32 v, u32 vo, u32 so) { __builtin_amdgcn_raw_buffer_store_b32(v, r, vo, so, AUX_SC1); }
DI void st128(rsrc_t r, v4u v, u32 vo, u32 so) { __builtin_amdgcn_raw_buffer_store_b128(v, r, vo, so, AUX_SC1); }
DI float asf(u32 x) { return __uint_as_float(x); }
DI u32 asu(float x) { return __float_as_uint(x); }
DI u32 cvtpk(float lo, float hi) { v2f v = {lo, hi}; return __builtin_bit_cast(u32, __builtin_convertvector(v, v2bf)); }
DI u32 dup(float x) { return cvtpk(x, x); }
DI float rbf(float x) { return asf(dup(x) & 0xffff0000u); }
template <int C> DI u32 dppu(u32 x) { return (u32)__builtin_amdgcn_update_dpp(0, (int)x, C, 0xf, 0xf, true); }
template <int C> DI float dppf(float x) { return asf(dppu<C>(asu(x))); }
constexpr int ROR8 = 0x128, ROR4 = 0x124, ROR2 = 0x122, ROR1 = 0x121;
constexpr int QP(int a, int b, int c, int d) { return a | (b << 2) | (c << 4) | (d << 6); }
DI float sw32add(float a, float b) { auto r = __builtin_amdgcn_permlane32_swap(asu(a), asu(b), false, false); return asf(r[0]) + asf(r[1]); }
DI float sw16add(float a, float b) { auto r = __builtin_amdgcn_permlane16_swap(asu(a), asu(b), false, false); return asf(r[0]) + asf(r[1]); }
DI float sw32max(float a) { auto r = __builtin_amdgcn_permlane32_swap(asu(a), asu(a), false, false); return fmaxf(asf(r[0]), asf(r[1])); }
DI float sw16max(float a) { auto r = __builtin_amdgcn_permlane16_swap(asu(a), asu(a), false, false); return fmaxf(asf(r[0]), asf(r[1])); }
DI float tred4(float x, float y, float z, float w) { return sw16add(sw32add(x, z), sw32add(y, w)); }
DI float rowsum(float v) { v += dppf<ROR8>(v); v += dppf<ROR4>(v); v += dppf<ROR2>(v); v += dppf<ROR1>(v); return v; }
DI float rowmax(float v) { v = fmaxf(v, dppf<ROR8>(v)); v = fmaxf(v, dppf<ROR4>(v)); v = fmaxf(v, dppf<ROR2>(v)); v = fmaxf(v, dppf<ROR1>(v)); return v; }
DI float wavesum(float v) { v = rowsum(v); v = sw16add(v, v); return sw32add(v, v); }
DI u32 enc(float v, u32 T) { return (asu(v) & 0xffffff00u) | T; }
DI void cfence() { asm volatile("" ::: "memory"); }
DI void bar() { asm volatile("s_waitcnt lgkmcnt(0)\n\ts_barrier" ::: "memory"); }
DI v4f mfma32(v4u a, v4u b, v4f c) { return __builtin_amdgcn_mfma_f32_16x16x32_bf16(__builtin_bit_cast(v8bf, a), __builtin_bit_cast(v8bf, b), c, 0, 0, 0); }
DI v4f mfma16(v2u a, v2u b, v4f c) { return __builtin_amdgcn_mfma_f32_16x16x16bf16_1k(__builtin_bit_cast(v4s, a), __builtin_bit_cast(v4s, b), c, 0, 0, 0); }
DI float silu_bf(u32 h) { float v = asf(h << 16); return v * __builtin_amdgcn_rcpf(1.f + __builtin_amdgcn_exp2f(-1.4426950408889634f * v)); }
DI float dot2(u32 a, u32 b, float c) { return __builtin_amdgcn_fdot2_f32_bf16(__builtin_bit_cast(v2bf, a), __builtin_bit_cast(v2bf, b), c, false); }
template <int N> DI void slp() { if (N > 0) __builtin_amdgcn_s_sleep(N); }

DI u32 untag(v4u& y, u32 T) { y.x ^= T; y.y ^= T; y.z ^= T; y.w ^= T; return y.x | y.y | y.z | y.w; }
template <int N> DI void gather(v4u (&y)[N], rsrc_t ws, const u32 (&vo)[N], u32 so, u32 T, bool have) {
  if (__builtin_amdgcn_ballot_w64(!have) == 0) return;
#pragma unroll 1
  for (u32 spin = 0; spin < SPIN_MAX; ++spin) {
    cfence();
#pragma unroll
    for (int k = 0; k < N; ++k) y[k] = ld128<AUX_SC1>(ws, vo[k], so);
    u32 bad = 0;
#pragma unroll
    for (int k = 0; k < N; ++k) bad |= untag(y[k], T);
    if (__builtin_amdgcn_ballot_w64((bad & 255u) != 0) == 0) break;
  }
}

#define STAMP(k) do {} while (0)

constexpr u32 L_RA = 0, L_RB = 2048, L_RM = 6144, L_RD = 8192;
constexpr u32 L_Q = 0, L_ML = 1024, L_X1 = 1536, L_H2 = 2560, L_G = 3584, L_RF = 4096, L_O = 8192, L_W = 24576;
constexpr u32 LDS_BYTES = 84 * 1024;

__global__ void __launch_bounds__(512, 2)
fused(const u16* __restrict__ xin, const u16* __restrict__ kc, const u16* __restrict__ vc, const u16* __restrict__ wq,
      const u16* __restrict__ wo, const u16* __restrict__ wg, const u16* __restrict__ wu, const u16* __restrict__ wd,
      const u16* __restrict__ cin, const u16* __restrict__ wmod, u16* __restrict__ out, char* wsp) {
  __shared__ __attribute__((aligned(16))) char lds[LDS_BYTES];
  STAMP(0);
  const u32 tst = (u32)__builtin_amdgcn_s_memrealtime();
  const int tid = threadIdx.x;
  const int w = __builtin_amdgcn_readfirstlane(tid >> 6);
  const int l = tid & 63, g = l >> 4, c = l & 15;
  const int xcd = blockIdx.x & 7, role = (blockIdx.x >> 3) & 1, i = blockIdx.x >> 4;
  const int idx = xcd * 16 + i;
  const rsrc_t ws = mk(wsp, WS_END);
  const rsrc_t rx = mk(xin, 1024);
  const u32 ep = ld32<AUX_SC1>(ws, 0, WS_EP);
  const int jj = (c & 3) + 4 * (c >> 3);
  const u32 hm = (jj & 1) ? 0xffff0000u : 0x0000ffffu;
  const v4u MK = {(jj >> 1) == 0 ? hm : 0u, (jj >> 1) == 1 ? hm : 0u, (jj >> 1) == 2 ? hm : 0u, (jj >> 1) == 3 ? hm : 0u};
  const v4f Z4 = {0.f, 0.f, 0.f, 0.f};
  float* ldsf = (float*)lds;

  if (role == 0) {
    const rsrc_t rmod = mk(wmod, 512 * 3072 * 2), rc = mk(cin, 1024), rwq = mk(wq, 512 * 4096 * 2), rwo = mk(wo, 4096 * 512 * 2);
    const int ka = idx >> 4, atc = idx & 15, kb = idx >> 5, head = idx & 31;
    const int a = (l >> 2) & 1;
    const u32 rowsel = 4 * (c >> 3) + g;
    const u32 cA = ld16(rc, (4 * a + g) * 2, (64 * ka + 8 * w) * 2);
    const v4u wA = ld128<AUX_NT>(rmod, rowsel * 6144 + (c & 7) * 16, (64 * ka + 8 * w) * 6144 + atc * 128);
    const v4u xv = ld128<0>(rx, l * 16, 0);
    const u32 xo = ld16(rx, (4 * (l & 3) + g) * 2, (128 * kb + 16 * w) * 2);
    const u32 cM0 = ld16(rc, (4 * a + g) * 2, (128 * kb + 16 * w) * 2);
    const u32 cM1 = ld16(rc, (4 * a + g) * 2 + 16, (128 * kb + 16 * w) * 2);
    cfence();
    v4u wB[4];
#pragma unroll
    for (int t = 0; t < 4; ++t) wB[t] = ld128<AUX_NT>(rwq, g * 8192 + c * 16, (128 * kb + 16 * w + 4 * t) * 8192 + head * 256);
    cfence();
    const u32 T = ((u32)__builtin_amdgcn_readfirstlane(ep) + 1u) & 255u;
    {
      const u32 sd = dup(silu_bf(cA));
      const v4f acc = mfma32(MK & sd, wA, Z4);
      auto p0 = __builtin_amdgcn_permlane16_swap(asu(acc.x), asu(acc.y), false, false);
      auto p1 = __builtin_amdgcn_permlane16_swap(asu(acc.z), asu(acc.w), false, false);
      const float s0 = asf(p0[0]) + dppf<ROR8>(asf(p0[1])), s1 = asf(p1[0]) + dppf<ROR8>(asf(p1[1]));
      if (c < 8) { float* p = ldsf + L_RA / 4 + w * 64 + 8 * c + (g & 1) + 4 * (g >> 1); p[0] = s0; p[2] = s1; }
    }
    STAMP(1);
    bar();
    if (w == 0) {
      const float* p = ldsf + L_RA / 4 + l;
      const float s = ((p[0] + p[64]) + (p[128] + p[192])) + ((p[256] + p[320]) + (p[384] + p[448]));
      st32(ws, enc(s, T), l * 4, WS_H1 + (ka * 16 + atc) * 256);
    }
    STAMP(2);
    float xn;
    {
      const u32 ONE = 0x3f803f80u;
      float s = dot2(xv.x, ONE, dot2(xv.y, ONE, 0.f)) + dot2(xv.z, ONE, dot2(xv.w, ONE, 0.f));
      const float mean = wavesum(s) * (1.f / 512.f);
      float q = 0.f;
      const u32 xs[4] = {xv.x, xv.y, xv.z, xv.w};
#pragma unroll
      for (int k = 0; k < 4; ++k) { const float d0 = asf(xs[k] << 16) - mean, d1 = asf(xs[k] & 0xffff0000u) - mean; q = fmaf(d0, d0, fmaf(d1, d1, q)); }
      const float var = rbf(wavesum(q) * (1.f / 512.f));
      const float den = rbf(__builtin_amdgcn_sqrtf(rbf(var + 1e-5f)));
      xn = rbf(rbf(asf(xo << 16) - rbf(mean)) * __builtin_amdgcn_rcpf(den));
    }
    const u32 sM0 = dup(silu_bf(cM0)), sM1 = dup(silu_bf(cM1));
    {
      slp<DLY_H1>();
      v4u y[1];
      const u32 vo[1] = {(u32)((l >> 2) * 2048 + (l & 3) * 16)};
      gather<1>(y, ws, vo, WS_H1 + (128 * kb + 16 * w) * 4, T, false);
      STAMP(3);
      float v = tred4(asf(y[0].x), asf(y[0].y), asf(y[0].z), asf(y[0].w));
      v = rbf(v + dppf<ROR8>(v));
      const float o = dppf<ROR4>(v);
      const float sa = a ? o : v, ca = a ? v : o;
      const u32 hd = dup(rbf(xn * rbf(1.f + ca)) + sa);
      v4f acc = Z4;
      acc = mfma32(MK & dppu<QP(0, 0, 0, 0)>(hd), wB[0], acc);
      acc = mfma32(MK & dppu<QP(1, 1, 1, 1)>(hd), wB[1], acc);
      acc = mfma32(MK & dppu<QP(2, 2, 2, 2)>(hd), wB[2], acc);
      acc = mfma32(MK & dppu<QP(3, 3, 3, 3)>(hd), wB[3], acc);
      if ((g & 1) == 0) *(v4f*)(ldsf + L_RB / 4 + w * 128 + 8 * c + 2 * g) = acc;
    }
    STAMP(4);
    bar();
    if (w < 2) {
      const float* p = ldsf + L_RB / 4 + tid;
      const float s = ((p[0] + p[128]) + (p[256] + p[384])) + ((p[512] + p[640]) + (p[768] + p[896]));
      st32(ws, enc(s, T), tid * 4, WS_H2 + kb * 16384 + head * 512);
    }
    asm volatile("s_barrier" ::: "memory");
    STAMP(5);
    const int I = idx >> 3, J = idx & 7;
    v4u wM[2], wD[4];
#pragma unroll
    for (int t = 0; t < 2; ++t) wM[t] = ld128<AUX_NT>(rmod, rowsel * 6144 + (c & 7) * 16, (128 * kb + 16 * w + 8 * t) * 6144 + 2048 + head * 128);
#pragma unroll
    for (int t = 0; t < 4; ++t) wD[t] = ld128<AUX_NT>(rwo, (16 * (c >> 3) + g) * 1024 + (c & 7) * 16, (256 * I + 32 * w + 4 * t) * 1024 + J * 128);
    cfence();
    u32 pf;
    {
      const rsrc_t rf = mk((w & 4) ? wu : wg, 512 * 1792 * 2);
      pf = ld32<0>(rf, (l >> 2) * 3584 + (l & 3) * 128, (i * 32 + (w & 1) * 16) * 3584 + ((xcd * 448) & ~127));
      if (w & 2) pf = 0;
    }
    cfence();
    {
      v4f acc = mfma32(MK & sM0, wM[0], Z4);
      acc = mfma32(MK & sM1, wM[1], acc);
      auto p0 = __builtin_amdgcn_permlane16_swap(asu(acc.x), asu(acc.y), false, false);
      auto p1 = __builtin_amdgcn_permlane16_swap(asu(acc.z), asu(acc.w), false, false);
      const float s0 = asf(p0[0]) + dppf<ROR8>(asf(p0[1])), s1 = asf(p1[0]) + dppf<ROR8>(asf(p1[1]));
      if (c < 8) { float* p = ldsf + L_RM / 4 + w * 64 + 8 * c + (g & 1) + 4 * (g >> 1); p[0] = s0; p[2] = s1; }
    }
    bar();
    if (w == 1) {
      const float* p = ldsf + L_RM / 4 + l;
      const float s = ((p[0] + p[64]) + (p[128] + p[192])) + ((p[256] + p[320]) + (p[384] + p[448]));
      st32(ws, enc(s, T), l * 4, WS_MR + kb * 8192 + head * 256);
    }
    STAMP(6);
    const int gk = I >> 1, hl = 2 * (I & 1) + (w >> 2);
    float f0, f1;
    {
      v4u y[1];
      const u32 vo[1] = {(u32)(c * 128)};
      gather<1>(y, ws, vo, WS_ST + gk * 2048 + hl * 16, T, false);
      const float m = asf(y[0].x), M = rowmax(m);
      const float e = __builtin_amdgcn_exp2f(m - M);
      const float f = e * __builtin_amdgcn_rcpf(rowsum(e * asf(y[0].y)));
      f0 = asf(__builtin_amdgcn_ds_bpermute((l >> 3) * 4, asu(f)));
      f1 = asf(__builtin_amdgcn_ds_bpermute((l >> 3) * 4 + 32, asu(f)));
    }
    STAMP(7);
    float ga = 0.f;
    if (w == 0) {
      v4u y[1];
      const u32 vo[1] = {(u32)(g * 8192 + c * 16)};
      gather<1>(y, ws, vo, WS_MR + J * 256, T, false);
      ga = rbf(tred4(asf(y[0].x), asf(y[0].y), asf(y[0].z), asf(y[0].w)));
    }
    STAMP(8);
    {
      slp<DLY_H3>();
      v4u y[2];
      const u32 vo[2] = {(u32)((l >> 3) * 2048 + (l & 7) * 16), (u32)((l >> 3) * 2048 + (l & 7) * 16 + 16384)};
      gather<2>(y, ws, vo, WS_H3 + gk * 32768 + hl * 512 + (w & 3) * 128, T, false);
      STAMP(9);
      float v = tred4(fmaf(f0, asf(y[0].x), f1 * asf(y[1].x)), fmaf(f0, asf(y[0].y), f1 * asf(y[1].y)),
                      fmaf(f0, asf(y[0].z), f1 * asf(y[1].z)), fmaf(f0, asf(y[0].w), f1 * asf(y[1].w)));
      const u32 od = dup(v + dppf<ROR8>(v));
      v4f acc = Z4;
      acc = mfma32(MK & dppu<QP(0, 0, 0, 0)>(od), wD[0], acc);
      acc = mfma32(MK & dppu<QP(1, 1, 1, 1)>(od), wD[1], acc);
      acc = mfma32(MK & dppu<QP(2, 2, 2, 2)>(od), wD[2], acc);
      acc = mfma32(MK & dppu<QP(3, 3, 3, 3)>(od), wD[3], acc);
      auto p0 = __builtin_amdgcn_permlane16_swap(asu(acc.x), asu(acc.y), false, false);
      auto p1 = __builtin_amdgcn_permlane16_swap(asu(acc.z), asu(acc.w), false, false);
      const float s0 = asf(p0[0]) + dppf<ROR8>(asf(p0[1])), s1 = asf(p1[0]) + dppf<ROR8>(asf(p1[1]));
      if (c < 8) { float* p = ldsf + L_RD / 4 + w * 64 + 8 * c + (g & 1) + 4 * (g >> 1); p[0] = s0; p[2] = s1; }
    }
    bar();
    if (w == 0) {
      const float* p = ldsf + L_RD / 4 + 4 * c + g;
      const float s = ((p[0] + p[64]) + (p[128] + p[192])) + ((p[256] + p[320]) + (p[384] + p[448]));
      st32(ws, enc(s * ga, T), (4 * c + g) * 4, WS_H4 + I * 2048 + J * 256);
    }
    STAMP(10);
    asm volatile("" :: "v"(pf));
  } else {
    const rsrc_t rk = mk(kc + ((size_t)xcd * 65536 + 61440) * 128, 4096 * 256), rv = mk(vc + ((size_t)xcd * 65536 + 61440) * 128, 4096 * 256);
    const int rot = w + i + xcd;
    const u32 voK = (c & 7) * 256 + (2 * g + (c >> 3)) * 16, voV = (4 * (g & 1) + (g >> 1)) * 256 + c * 16;
    const u32 sb0 = i * 65536 + w * 8192;
    v4u kr[4][2], vr[4][2];
#pragma unroll
    for (int q = 0; q < 4; ++q) {
      kr[q][0] = ld128<AUX_NT>(rk, voK, sb0 + ((q + rot) & 3) * 2048);
      kr[q][1] = ld128<AUX_NT>(rk, voK + 128, sb0 + ((q + rot) & 3) * 2048);
    }
    cfence();
#pragma unroll
    for (int q = 0; q < 2; ++q) {
      vr[q][0] = ld128<AUX_NT>(rv, voV, sb0 + ((q + rot) & 3) * 2048);
      vr[q][1] = ld128<AUX_NT>(rv, voV + 512, sb0 + ((q + rot) & 3) * 2048);
    }
    asm volatile("s_waitcnt vmcnt(4)" ::: "memory");
    STAMP(1);
    const u32 T = ((u32)__builtin_amdgcn_readfirstlane(ep) + 1u) & 255u;
    {
      v4u y[1];
      const u32 vo[1] = {(u32)(g * 16384 + c * 16)};
      y[0] = ld128<AUX_SC1>(ws, vo[0], WS_H2 + xcd * 2048 + w * 256);
      cfence();
#pragma unroll
      for (int q = 2; q < 4; ++q) {
        vr[q][0] = ld128<AUX_NT>(rv, voV, sb0 + ((q + rot) & 3) * 2048);
        vr[q][1] = ld128<AUX_NT>(rv, voV + 512, sb0 + ((q + rot) & 3) * 2048);
      }
      cfence();
      const bool ok = (untag(y[0], T) & 255u) == 0;
      STAMP(2);
      gather<1>(y, ws, vo, WS_H2 + xcd * 2048 + w * 256, T, ok);
      STAMP(3);
      const float qv = tred4(asf(y[0].x), asf(y[0].y), asf(y[0].z), asf(y[0].w));
      *(u16*)(lds + L_Q + (64 * w + 4 * c + g) * 2) = (u16)dup(qv);
    }
    bar();
    float sc[8];
    {
      const v4u q0 = *(const v4u*)(lds + L_Q + (c >> 2) * 256 + (2 * g + (c & 1)) * 16);
      const v4u q1 = *(const v4u*)(lds + L_Q + (c >> 2) * 256 + (2 * g + (c & 1)) * 16 + 128);
#pragma unroll
      for (int q = 0; q < 4; ++q) {
        const v4f d = mfma32(kr[q][1], q1, mfma32(kr[q][0], q0, Z4));
        auto p0 = __builtin_amdgcn_permlane32_swap(asu(d.x), asu(d.y), false, false);
        auto p1 = __builtin_amdgcn_permlane32_swap(asu(d.z), asu(d.w), false, false);
        const float s0 = asf(p0[0]) + dppf<QP(1, 1, 1, 1)>(asf(p0[1])), s1 = asf(p1[0]) + dppf<QP(1, 1, 1, 1)>(asf(p1[1]));
        const u32 r1 = dppu<QP(0, 0, 0, 0)>(cvtpk(s0, s1));
        const u32 r2 = cvtpk(asf(r1 << 16) * 0.08838834764831845f, asf(r1 & 0xffff0000u) * 0.08838834764831845f);
        sc[2 * q] = asf(r2 << 16) * 1.4426950408889634f;
        sc[2 * q + 1] = asf(r2 & 0xffff0000u) * 1.4426950408889634f;
      }
    }
    float mw = fmaxf(fmaxf(fmaxf(sc[0], sc[1]), fmaxf(sc[2], sc[3])), fmaxf(fmaxf(sc[4], sc[5]), fmaxf(sc[6], sc[7])));
    mw = sw32max(sw16max(mw));
    float lw = 0.f;
#pragma unroll
    for (int k = 0; k < 8; ++k) { sc[k] = __builtin_amdgcn_exp2f(sc[k] - mw); lw += sc[k]; }
    lw = sw16add(lw, lw); lw = sw32add(lw, lw);
    if (l < 16 && (c & 3) == 0) { ldsf[L_ML / 4 + (c >> 2) * 8 + w] = mw; ldsf[L_ML / 4 + 32 + (c >> 2) * 8 + w] = lw; }
    STAMP(4);
    bar();
    v2u pa[8];
    {
      const v4f m0 = *(const v4f*)(ldsf + L_ML / 4 + (c >> 2) * 8), m1 = *(const v4f*)(ldsf + L_ML / 4 + (c >> 2) * 8 + 4);
      float M = fmaxf(fmaxf(fmaxf(m0.x, m0.y), fmaxf(m0.z, m0.w)), fmaxf(fmaxf(m1.x, m1.y), fmaxf(m1.z, m1.w)));
      M = asf(asu(M) & 0xffffff00u);
      if (w == 0) {
        const v4f l0 = *(const v4f*)(ldsf + L_ML / 4 + 32 + (c >> 2) * 8), l1 = *(const v4f*)(ldsf + L_ML / 4 + 32 + (c >> 2) * 8 + 4);
        float L = l0.x * __builtin_amdgcn_exp2f(m0.x - M);
        L = fmaf(l0.y, __builtin_amdgcn_exp2f(m0.y - M), L); L = fmaf(l0.z, __builtin_amdgcn_exp2f(m0.z - M), L); L = fmaf(l0.w, __builtin_amdgcn_exp2f(m0.w - M), L);
        L = fmaf(l1.x, __builtin_amdgcn_exp2f(m1.x - M), L); L = fmaf(l1.y, __builtin_amdgcn_exp2f(m1.y - M), L); L = fmaf(l1.z, __builtin_amdgcn_exp2f(m1.z - M), L); L = fmaf(l1.w, __builtin_amdgcn_exp2f(m1.w - M), L);
        const v4u u = {enc(M, T), enc(L, T), enc(M, T), enc(L, T)};
        if (l < 32 && (c & 3) == 0) st128(ws, u, (c >> 2) * 16 + g * 64, WS_ST + idx * 128);
      }
      const float cw = __builtin_amdgcn_exp2f(mw - M);
      const u32 h4 = (c & 1) ? 0xffff0000u : 0x0000ffffu;
      const u32 MA0 = (c & 2) ? 0u : h4, MA1 = (c & 2) ? h4 : 0u;
#pragma unroll
      for (int k = 0; k < 8; ++k) { const u32 d = dup(sc[k] * cw); pa[k] = v2u{d & MA0, d & MA1}; }
    }
    STAMP(5);
    {
      v4f a0 = Z4, a1 = Z4;
#pragma unroll
      for (int k = 0; k < 8; ++k) {
        const v4u v = vr[k >> 1][k & 1];
        a0 = mfma16(pa[k], v2u{v.x, v.y}, a0);
        a1 = mfma16(pa[k], v2u{v.z, v.w}, a1);
      }
      v4f* p = (v4f*)(lds + L_O + w * 2048 + g * 512 + c * 32);
      p[0] = a0; p[1] = a1;
    }
    STAMP(6);
    bar();
    {
      const float* p = ldsf + L_O / 4 + tid;
      const float s = ((p[0] + p[512]) + (p[1024] + p[1536])) + ((p[2048] + p[2560]) + (p[3072] + p[3584]));
      st32(ws, enc(s, T), tid * 4, WS_H3 + idx * 2048);
    }
    asm volatile("s_barrier" ::: "memory");
    STAMP(7);
    if (i < 14) {
      const int e = xcd * 14 + i, kq = w & 3;
      const rsrc_t rf = mk((w & 4) ? wu : wg, 512 * 1792 * 2), rd = mk(wd, 1792 * 512 * 2);
      v4u wf[4], wdn[4];
#pragma unroll
      for (int t = 0; t < 4; ++t) wf[t] = ld128<0>(rf, (l >> 1) * 3584 + (l & 1) * 16, (128 * kq + 32 * t) * 3584 + e * 32);
      if (w < 4) {
#pragma unroll
        for (int t = 0; t < 4; ++t) wdn[t] = ld128<AUX_NT>(rd, g * 1024 + c * 16, (16 * e + 4 * t) * 1024 + w * 256);
      }
      const u32 xo = ld16(rx, (4 * c + g) * 2, w * 128);
      cfence();
      float sbv, cb1;
      {
        v4u y[2];
        const u32 vo[2] = {(u32)(g * 8192 + c * 16), (u32)(g * 8192 + c * 16 + 2048)};
        gather<2>(y, ws, vo, WS_MR + 2048 + w * 256, T, false);
        sbv = rbf(tred4(asf(y[0].x), asf(y[0].y), asf(y[0].z), asf(y[0].w)));
        cb1 = rbf(1.f + rbf(tred4(asf(y[1].x), asf(y[1].y), asf(y[1].z), asf(y[1].w))));
      }
      v4u bw[4];
#pragma unroll
      for (int t = 0; t < 4; ++t) *(v4u*)(lds + L_W + w * 4096 + t * 1024 + l * 16) = wf[t];
#pragma unroll
      for (int t = 0; t < 4; ++t) {
        typedef __attribute__((address_space(3))) v4s* lp;
        const char* b = lds + L_W + w * 4096 + t * 1024 + (8 * g + (c >> 2)) * 32 + (c & 3) * 8;
        const v2u lo = __builtin_bit_cast(v2u, __builtin_amdgcn_ds_read_tr16_b64_v4i16((lp)b));
        const v2u hi = __builtin_bit_cast(v2u, __builtin_amdgcn_ds_read_tr16_b64_v4i16((lp)(b + 128)));
        bw[t] = v4u{lo.x, lo.y, hi.x, hi.y};
      }
      STAMP(8);
      float x1;
      {
        slp<DLY_H4>();
        v4u y[4];
        const u32 v0 = g * 2048 + c * 16;
        const u32 vo[4] = {v0, v0 + 8192, v0 + 16384, v0 + 24576};
        gather<4>(y, ws, vo, WS_H4 + w * 256, T, false);
        STAMP(9);
        const float s = tred4((asf(y[0].x) + asf(y[1].x)) + (asf(y[2].x) + asf(y[3].x)), (asf(y[0].y) + asf(y[1].y)) + (asf(y[2].y) + asf(y[3].y)),
                              (asf(y[0].z) + asf(y[1].z)) + (asf(y[2].z) + asf(y[3].z)), (asf(y[0].w) + asf(y[1].w)) + (asf(y[2].w) + asf(y[3].w)));
        const u32 xd = dup(asf(xo << 16) + rbf(s));
        x1 = asf(xd & 0xffff0000u);
        *(u16*)(lds + L_X1 + (64 * w + 4 * c + g) * 2) = (u16)xd;
      }
      bar();
      {
        const v4u xa = *(const v4u*)(lds + L_X1 + l * 16);
        const u32 ONE = 0x3f803f80u;
        const float s = dot2(xa.x, ONE, dot2(xa.y, ONE, 0.f)) + dot2(xa.z, ONE, dot2(xa.w, ONE, 0.f));
        const float q = dot2(xa.x, xa.x, dot2(xa.y, xa.y, 0.f)) + dot2(xa.z, xa.z, dot2(xa.w, xa.w, 0.f));
        float r = sw32add(s, q);
        r = rowsum(sw16add(r, r));
        const float mean = asf(__builtin_amdgcn_readlane(asu(r), 0)) * (1.f / 512.f);
        const float msq = asf(__builtin_amdgcn_readlane(asu(r), 32)) * (1.f / 512.f);
        const float var = rbf(fmaxf(msq - mean * mean, 0.f));
        const float den = rbf(__builtin_amdgcn_sqrtf(rbf(var + 1e-5f)));
        const float xn = rbf(rbf(x1 - rbf(mean)) * __builtin_amdgcn_rcpf(den));
        *(u16*)(lds + L_H2 + (64 * w + 4 * c + g) * 2) = (u16)dup(rbf(xn * cb1) + sbv);
      }
      bar();
      {
        v4f acc = Z4;
#pragma unroll
        for (int t = 0; t < 4; ++t) acc = mfma32(*(const v4u*)(lds + L_H2 + (128 * kq + 32 * t + 8 * g) * 2), bw[t], acc);
        if (l < 16) ldsf[L_G / 4 + (w >> 2) * 64 + c * 4 + kq] = acc.x;
      }
      STAMP(10);
      bar();
      if (w < 4) {
        const int col = 4 * (l & 3) + g;
        const v4f gp = *(const v4f*)(ldsf + L_G / 4 + col * 4), up = *(const v4f*)(ldsf + L_G / 4 + 64 + col * 4);
        const float gt = rbf((gp.x + gp.y) + (gp.z + gp.w)), uu = rbf((up.x + up.y) + (up.z + up.w));
        const float z = gt * fmaf(gt * gt, 0.044715f, 1.f) * (-2.f * 0.7978845608028654f * 1.4426950408889634f);
        const float ge = rbf(gt * __builtin_amdgcn_rcpf(1.f + __builtin_amdgcn_exp2f(z)));
        const u32 hd = dup(ge * uu);
        v4f acc = Z4;
        acc = mfma32(MK & dppu<QP(0, 0, 0, 0)>(hd), wdn[0], acc);
        acc = mfma32(MK & dppu<QP(1, 1, 1, 1)>(hd), wdn[1], acc);
        acc = mfma32(MK & dppu<QP(2, 2, 2, 2)>(hd), wdn[2], acc);
        acc = mfma32(MK & dppu<QP(3, 3, 3, 3)>(hd), wdn[3], acc);
        const v4u u = {enc(acc.x, T), enc(acc.y, T), enc(acc.z, T), enc(acc.w, T)};
        if ((g & 1) == 0) st128(ws, u, c * 32 + g * 8, WS_H5 + e * 2048 + w * 512);
      }
      STAMP(11);
    } else {
      const int r = xcd * 2 + (i - 14);
      const int col = 4 * (l & 7) + g;
      float x1 = 0.f, gb = 0.f;
      if (w == 0) {
        const u32 xo = ld16(rx, col * 2, r * 64);
        v4u y[1];
        const u32 vo[1] = {(u32)(g * 8192 + (l & 7) * 16)};
        gather<1>(y, ws, vo, WS_MR + 6144 + r * 128, T, false);
        gb = rbf(tred4(asf(y[0].x), asf(y[0].y), asf(y[0].z), asf(y[0].w)));
        v4u z[2];
        const u32 vz[2] = {(u32)((l >> 3) * 2048 + (l & 7) * 16), (u32)((l >> 3) * 2048 + (l & 7) * 16 + 16384)};
        STAMP(8);
        gather<2>(z, ws, vz, WS_H4 + r * 128, T, false);
        STAMP(9);
        float s = tred4(asf(z[0].x) + asf(z[1].x), asf(z[0].y) + asf(z[1].y), asf(z[0].z) + asf(z[1].z), asf(z[0].w) + asf(z[1].w));
        s += dppf<ROR8>(s);
        x1 = rbf(asf(xo << 16) + rbf(s));
      }
      bar();
      {
        slp<DLY_H5>();
        v4u y[2];
        const u32 v0 = (l >> 3) * 2048 + (l & 7) * 16;
        const bool two = l < 48;
        const u32 vo[2] = {v0, two ? v0 + 16384 : v0};
        gather<2>(y, ws, vo, WS_H5 + w * (14 * 2048) + r * 128, T, false);
        STAMP(10);
        const float k2 = two ? 1.f : 0.f;
        float s = tred4(fmaf(k2, asf(y[1].x), asf(y[0].x)), fmaf(k2, asf(y[1].y), asf(y[0].y)), fmaf(k2, asf(y[1].z), asf(y[0].z)), fmaf(k2, asf(y[1].w), asf(y[0].w)));
        s += dppf<ROR8>(s);
        if ((l & 8) == 0) ldsf[L_RF / 4 + w * 32 + col] = s;
      }
      bar();
      if (w == 0) {
        const float* p = ldsf + L_RF / 4 + col;
        const float s = ((p[0] + p[32]) + (p[64] + p[96])) + ((p[128] + p[160]) + (p[192] + p[224]));
        const u32 o = dup(x1 + rbf(gb * rbf(s)));
        if ((l & 8) == 0) out[r * 32 + col] = (u16)o;
        if (r == 0 && l == 0) st32(ws, T, 0, WS_EP);
      }
      STAMP(11);
    }
  }
  if (w == 0) __builtin_amdgcn_fence(__ATOMIC_RELEASE, "agent");
  if (PADALL || (blockIdx.x == 8 + 14 * 16 && w == 0)) { while ((u32)__builtin_amdgcn_s_memrealtime() - tst < PADD) __builtin_amdgcn_s_sleep(1); }
}

extern "C" void kernel_launch(void* const* d_in, const int* in_sizes, int n_in, void* d_out, int out_size, void* d_ws, size_t ws_size,
                              hipStream_t stream) {
  (void)in_sizes; (void)n_in; (void)out_size; (void)ws_size;
  fused<<<256, 512, 0, stream>>>((const u16*)d_in[0], (const u16*)d_in[1], (const u16*)d_in[2], (const u16*)d_in[3], (const u16*)d_in[4],
                                 (const u16*)d_in[6], (const u16*)d_in[7], (const u16*)d_in[8], (const u16*)d_in[9], (const u16*)d_in[10],
                                 (u16*)d_out, (char*)d_ws);
}
```

```cpp
#include <hip/hip_runtime.h>
#include <stdint.h>

typedef unsigned int u32;
typedef unsigned short u16;
typedef u32 v4u __attribute__((ext_vector_type(4)));
typedef u32 v2u __attribute__((ext_vector_type(2)));
typedef float v4f __attribute__((ext_vector_type(4)));
typedef float v2f __attribute__((ext_vector_type(2)));
typedef __bf16 v8bf __attribute__((ext_vector_type(8)));
typedef __bf16 v2bf __attribute__((ext_vector_type(2)));
typedef short v4s __attribute__((ext_vector_type(4)));
typedef __amdgpu_buffer_rsrc_t rsrc_t;
#define DI __device__ __forceinline__

#define PROF 0

constexpr u32 WS_EP = 0;
constexpr u32 WS_H1 = 4096;
constexpr u32 WS_H2 = WS_H1 + 65536;
constexpr u32 WS_ST = WS_H2 + 131072;
constexpr u32 WS_MR = WS_ST + 16384;
constexpr u32 WS_H4 = WS_MR + 16384;
constexpr u32 WS_H3 = WS_H4 + 32768;
constexpr u32 WS_H5 = WS_H3 + 262144;
constexpr u32 WS_END = WS_H5 + 56 * 2048;
constexpr u32 WS_PROF = 1u << 20;

constexpr int AUX_NT = 2, AUX_SC1 = 16;
constexpr u32 SPIN_MAX = 1u << 16;
constexpr u32 TAGM = 1023u;

#define DLY_KV 0
#define DLY_H1 0
#define T_Q 0
#define DLY_W2 0
#define DLY_H3 0
#define DLY_H4 0
#define DLY_H5 0
#define AUX_GU 0
#define CODE_PF 0

DI rsrc_t mk(const void* p, u32 bytes) { return __builtin_amdgcn_make_buffer_rsrc((void*)p, 0, bytes, 0x00020000); }
template <int AUX> DI v4u ld128(rsrc_t r, u32 vo, u32 so) { return __builtin_amdgcn_raw_buffer_load_b128(r, vo, so, AUX); }
template <int AUX> DI u32 ld32(rsrc_t r, u32 vo, u32 so) { return __builtin_amdgcn_raw_buffer_load_b32(r, vo, so, AUX); }
DI u32 ld16(rsrc_t r, u32 vo, u32 so) { return (u32)(u16)__builtin_amdgcn_raw_buffer_load_b16(r, vo, so, 0); }
DI void st32(rsrc_t r, u32 v, u32 vo, u32 so) { __builtin_amdgcn_raw_buffer_store_b32(v, r, vo, so, AUX_SC1); }
DI void st128(rsrc_t r, v4u v, u32 vo, u32 so) { __builtin_amdgcn_raw_buffer_store_b128(v, r, vo, so, AUX_SC1); }
DI float asf(u32 x) { return __uint_as_float(x); }
DI u32 asu(float x) { return __float_as_uint(x); }
DI u32 cvtpk(float lo, float hi) { v2f v = {lo, hi}; return __builtin_bit_cast(u32, __builtin_convertvector(v, v2bf)); }
DI u32 dup(float x) { return cvtpk(x, x); }
DI float rbf(float x) { return asf(dup(x) & 0xffff0000u); }
template <int C> DI u32 dppu(u32 x) { return (u32)__builtin_amdgcn_update_dpp(0, (int)x, C, 0xf, 0xf, true); }
template <int C> DI float dppf(float x) { return asf(dppu<C>(asu(x))); }
template <int C, int RM, int BM> DI u32 dppm(u32 old, u32 x) { return (u32)__builtin_amdgcn_update_dpp((int)old, (int)x, C, RM, BM, false); }
constexpr int ROR8 = 0x128, ROR4 = 0x124, HMIR = 0x141;
constexpr int QP(int a, int b, int c, int d) { return a | (b << 2) | (c << 4) | (d << 6); }
DI float sw32add(float a, float b) { auto r = __builtin_amdgcn_permlane32_swap(asu(a), asu(b), false, false); return asf(r[0]) + asf(r[1]); }
DI float sw16add(float a, float b) { auto r = __builtin_amdgcn_permlane16_swap(asu(a), asu(b), false, false); return asf(r[0]) + asf(r[1]); }
DI float sw32max(float a) { auto r = __builtin_amdgcn_permlane32_swap(asu(a), asu(a), false, false); return fmaxf(asf(r[0]), asf(r[1])); }
DI float sw16max(float a) { auto r = __builtin_amdgcn_permlane16_swap(asu(a), asu(a), false, false); return fmaxf(asf(r[0]), asf(r[1])); }
DI float tred4(float x, float y, float z, float w) { return sw16add(sw32add(x, z), sw32add(y, w)); }
DI float rowsum(float v) { v += dppf<ROR8>(v); v += dppf<ROR4>(v); v += dppf<QP(2, 3, 0, 1)>(v); v += dppf<QP(1, 0, 3, 2)>(v); return v; }
DI float wavesum(float v) { v = rowsum(v); v = sw16add(v, v); return sw32add(v, v); }
DI u32 enc(float v, u32 T) { return (asu(v) & ~TAGM) | T; }
DI v4u enc4(v4f v, u32 T) { return v4u{enc(v.x, T), enc(v.y, T), enc(v.z, T), enc(v.w, T)}; }
DI v4f asf4(v4u v) { return __builtin_bit_cast(v4f, v); }
DI void cfence() { asm volatile("" ::: "memory"); }
DI void bar() { asm volatile("s_waitcnt lgkmcnt(0)\n\ts_barrier" ::: "memory"); }
DI v4f mfma32(v4u a, v4u b, v4f c) { return __builtin_amdgcn_mfma_f32_16x16x32_bf16(__builtin_bit_cast(v8bf, a), __builtin_bit_cast(v8bf, b), c, 0, 0, 0); }
DI v4f mfma16(v2u a, v2u b, v4f c) { return __builtin_amdgcn_mfma_f32_16x16x16bf16_1k(__builtin_bit_cast(v4s, a), __builtin_bit_cast(v4s, b), c, 0, 0, 0); }
DI float silu_bf(u32 h) {
  const float v = asf(h << 16), d = 1.f + __builtin_amdgcn_exp2f(fminf(-1.4426950408889634f * v, 126.f)), r = __builtin_amdgcn_rcpf(d), q = v * r;
  return fmaf(fmaf(-d, q, v), r, q);
}
DI float dot2(u32 a, u32 b, float c) { return __builtin_amdgcn_fdot2_f32_bf16(__builtin_bit_cast(v2bf, a), __builtin_bit_cast(v2bf, b), c, false); }
template <int N> DI void slp() { if (N > 0) __builtin_amdgcn_s_sleep(N); }
DI u32 now() { return (u32)__builtin_amdgcn_s_memrealtime(); }
DI v4f gemv4(v4u MK, u32 act, const v4u (&wt)[4]) {
  const v4f Z = {0.f, 0.f, 0.f, 0.f};
  v4f a0 = mfma32(MK & dppu<QP(0, 0, 0, 0)>(act), wt[0], Z);
  v4f a1 = mfma32(MK & dppu<QP(1, 1, 1, 1)>(act), wt[1], Z);
  a0 = mfma32(MK & dppu<QP(2, 2, 2, 2)>(act), wt[2], a0);
  a1 = mfma32(MK & dppu<QP(3, 3, 3, 3)>(act), wt[3], a1);
  return a0 + a1;
}
DI void half2(v4f acc, float* p, int c) {
  auto p0 = __builtin_amdgcn_permlane16_swap(asu(acc.x), asu(acc.y), false, false);
  auto p1 = __builtin_amdgcn_permlane16_swap(asu(acc.z), asu(acc.w), false, false);
  const float s0 = asf(p0[0]) + dppf<ROR8>(asf(p0[1])), s1 = asf(p1[0]) + dppf<ROR8>(asf(p1[1]));
  if (c < 8) { p[0] = s0; p[2] = s1; }
}
DI float half1(float x) { auto p = __builtin_amdgcn_permlane16_swap(asu(x), asu(x), false, false); return x + dppf<ROR8>(asf(p[1])); }

template <int N, bool WIDE> DI bool arrived(v4u (&y)[N], u32 T) {
  u32 bad = 0;
#pragma unroll
  for (int k = 0; k < N; ++k) {
    if (WIDE) bad |= (y[k].y ^ T) | (y[k].w ^ T);
    else { y[k] ^= T; bad |= (y[k].x | y[k].y) | (y[k].z | y[k].w); }
  }
  if (!WIDE) bad &= TAGM;
  return __builtin_amdgcn_ballot_w64(bad != 0) == 0;
}
template <int N, bool WIDE> DI void gather(v4u (&y)[N], rsrc_t ws, const u32 (&vo)[N], u32 so, u32 T) {
#pragma unroll 1
  for (u32 spin = 0; spin < SPIN_MAX; ++spin) {
    cfence();
#pragma unroll
    for (int k = 0; k < N; ++k) y[k] = ld128<AUX_SC1>(ws, vo[k], so);
    if (arrived<N, WIDE>(y, T)) break;
  }
}

#define STAMP(k) do {} while (0)

constexpr u32 L_RA = 0, L_RB = 2048, L_RM = 6144, L_G1 = 8192, L_G2 = 10240, L_G3 = 12288, L_SX = 14336, L_W = 16384;
constexpr u32 L_Q = 0, L_ML = 1024, L_RD = 2048, L_O = 8192;
constexpr u32 LDS_BYTES = 84 * 1024;

__global__ void __launch_bounds__(512, 2)
fused(const u16* __restrict__ xin, const u16* __restrict__ kc, const u16* __restrict__ vc, const u16* __restrict__ wq,
      const u16* __restrict__ wo, const u16* __restrict__ wg, const u16* __restrict__ wu, const u16* __restrict__ wd,
      const u16* __restrict__ cin, const u16* __restrict__ wmod, u16* __restrict__ out, char* wsp) {
  __shared__ __attribute__((aligned(16))) char lds[LDS_BYTES];
  const u32 t_start = now();
  STAMP(0);
  const int tid = threadIdx.x;
  const int w = __builtin_amdgcn_readfirstlane(tid >> 6);
  const int l = tid & 63, g = l >> 4, c = l & 15;
  const int xcd = blockIdx.x & 7, role = (blockIdx.x >> 3) & 1, i = blockIdx.x >> 4;
  const int idx = xcd * 16 + i;
  asm volatile("" :: "s"(xin), "s"(kc), "s"(vc), "s"(wq), "s"(wo), "s"(wg), "s"(wu), "s"(wd), "s"(cin), "s"(wmod), "s"(out), "s"(wsp));
  STAMP(15);
  const rsrc_t ws = mk(wsp, WS_END);
  const rsrc_t rx = mk(xin, 1024), rc = mk(cin, 1024), rmod = mk(wmod, 512 * 3072 * 2);
  const int a = (l >> 2) & 1;
  const int kb = idx >> 5, head = idx & 31, r0 = 128 * kb + 16 * w;
  const int rot = w + i + xcd;
  const u32 voV = (4 * (g & 1) + (g >> 1)) * 256 + c * 16;
  const u32 sb0 = i * 65536 + w * 8192;
  const rsrc_t rv = mk(vc + ((size_t)xcd * 65536 + 61440) * 128, 4096 * 256);
  v4u R[12];
  u32 cA = 0, xo = 0, pf = 0;
  if (role == 0) {
    const rsrc_t rwq = mk(wq, 512 * 4096 * 2);
    R[4] = ld128<AUX_NT>(rmod, (4 * (c >> 3) + g) * 6144 + (c & 7) * 16, (64 * xcd + 8 * w) * 6144 + i * 128);
    cA = ld16(rc, (4 * a + g) * 2, (64 * xcd + 8 * w) * 2);
    cfence();
#pragma unroll
    for (int t = 0; t < 4; ++t) R[t] = ld128<AUX_NT>(rwq, (8 * (g >> 1) + (g & 1)) * 8192 + c * 16, (r0 + 2 * t) * 8192 + head * 256);
    R[5] = ld128<0>(rx, l * 16, 0);
    xo = ld16(rx, (2 * (c & 7) + (g & 1)) * 2, r0 * 2);
  } else {
    slp<DLY_KV>();
    const rsrc_t rk = mk(kc + ((size_t)xcd * 65536 + 61440) * 128, 4096 * 256);
    const u32 voK = (c & 7) * 256 + (2 * g + (c >> 3)) * 16;
#pragma unroll
    for (int q = 0; q < 4; ++q) {
      R[2 * q] = ld128<AUX_NT>(rk, voK, sb0 + ((q + rot) & 3) * 2048);
      R[2 * q + 1] = ld128<AUX_NT>(rk, voK + 128, sb0 + ((q + rot) & 3) * 2048);
    }
    cfence();
#pragma unroll
    for (int q = 0; q < 2; ++q) {
      R[8 + 2 * q] = ld128<AUX_NT>(rv, voV, sb0 + ((q + rot) & 3) * 2048);
      R[9 + 2 * q] = ld128<AUX_NT>(rv, voV + 512, sb0 + ((q + rot) & 3) * 2048);
    }
  }
  const u32 ep = ld32<AUX_SC1>(ws, 0, WS_EP);
  cfence();
  STAMP(14);
  int role2 = role;
  asm volatile("" : "+s"(role2));
  const int jj = (c & 3) + 4 * (c >> 3);
  const u32 hm = (c & 1) ? 0xffff0000u : 0x0000ffffu;
  const v4u MK = {(jj >> 1) == 0 ? hm : 0u, (jj >> 1) == 1 ? hm : 0u, (jj >> 1) == 2 ? hm : 0u, (jj >> 1) == 3 ? hm : 0u};
  const v4f Z4 = {0.f, 0.f, 0.f, 0.f};
  float* ldsf = (float*)lds;
  const int col2 = 8 * c + (g & 1) + 4 * (g >> 1);

  if (role2 == 0) {
    half2(mfma32(MK & dup(silu_bf(cA)), R[4], Z4), ldsf + L_RA / 4 + w * 64 + col2, c);
    STAMP(1);
    const u32 T = (u32)__builtin_amdgcn_readfirstlane(ep) + 1u, TP = T & TAGM;
    bar();
    if (w == 0) {
      const v2f* p = (const v2f*)(ldsf + L_RA / 4 + (l >> 5) * 256 + (l & 31) * 2);
      const v2f s = (p[0] + p[32]) + (p[64] + p[96]);
      const v4u u = {asu(sw32add(s.x, s.x)), T, asu(sw32add(s.y, s.y)), T};
      if (l < 32) st128(ws, u, l * 16, WS_H1 + idx * 512);
    }
    STAMP(2);
    float xn;
    {
      const v4u xv = R[5];
      const u32 ONE = 0x3f803f80u;
      float s = dot2(xv.x, ONE, dot2(xv.y, ONE, 0.f)) + dot2(xv.z, ONE, dot2(xv.w, ONE, 0.f));
      const float mean = wavesum(s) * (1.f / 512.f);
      float q = 0.f;
      const u32 xs[4] = {xv.x, xv.y, xv.z, xv.w};
#pragma unroll
      for (int k = 0; k < 4; ++k) { const float d0 = asf(xs[k] << 16) - mean, d1 = asf(xs[k] & 0xffff0000u) - mean; q = fmaf(d0, d0, fmaf(d1, d1, q)); }
      const float var = rbf(wavesum(q) * (1.f / 512.f));
      const float den = rbf(__builtin_amdgcn_sqrtf(rbf(var + 1e-5f)));
      xn = rbf(rbf(asf(xo << 16) - rbf(mean)) / den);
    }
    {
      slp<DLY_H1>();
      v4u y[2];
      const u32 v0 = (2 * g + (c >> 3)) * 8192 + (c & 7) * 16;
      const u32 vo[2] = {v0, v0 + 4096};
      gather<2, true>(y, ws, vo, WS_H1 + r0 * 8, T);
      STAMP(3);
      float v = tred4(asf(y[0].x), asf(y[0].z), asf(y[1].x), asf(y[1].z));
      v = rbf(v + dppf<ROR8>(v));
      auto sc = __builtin_amdgcn_permlane32_swap(asu(v), asu(v), false, false);
      u32 hd = dup(rbf(xn * rbf(1.f + asf(sc[1]))) + asf(sc[0]));
      hd = dppm<ROR4, 0xc, 0xf>(hd, hd);
      const v4u wt[4] = {R[0], R[1], R[2], R[3]};
      const v4f acc = gemv4(MK, hd, wt);
      if ((g & 1) == 0) *(v4f*)(ldsf + L_RB / 4 + w * 128 + 8 * c + 2 * g) = acc;
    }
    STAMP(4);
    bar();
    if (w < 2) {
      const v2f* p = (const v2f*)(ldsf + L_RB / 4 + (l >> 5) * 512 + w * 64 + (l & 31) * 2);
      const v2f s = (p[0] + p[64]) + (p[128] + p[192]);
      const v4u u = {asu(sw32add(s.x, s.x)), T, asu(sw32add(s.y, s.y)), T};
      if (l < 32) st128(ws, u, l * 16, WS_H2 + kb * 32768 + head * 1024 + w * 512);
    }
    asm volatile("s_barrier" ::: "memory");
    STAMP(5);
    slp<DLY_W2>();
    const u32 rowa = 16 * a + 4 * (c & 3) + g;
    const u32 rowl = 16 * (c >> 3) + g;
    if (i < 7) {
      const int e = xcd * 7 + i;
      const rsrc_t rg = mk(wg, 512 * 1792 * 2), ru = mk(wu, 512 * 1792 * 2), rd = mk(wd, 1792 * 512 * 2);
      v4u wf[8], wdn[4];
#pragma unroll
      for (int t = 0; t < 4; ++t) wf[t] = ld128<AUX_GU>(rg, (l >> 2) * 3584 + (l & 3) * 16, (64 * w + 16 * t) * 3584 + e * 64);
#pragma unroll
      for (int t = 0; t < 4; ++t) wf[4 + t] = ld128<AUX_GU>(ru, (l >> 2) * 3584 + (l & 3) * 16, (64 * w + 16 * t) * 3584 + e * 64);
#pragma unroll
      for (int t = 0; t < 4; ++t) wdn[t] = ld128<AUX_NT>(rd, rowl * 1024 + (c & 7) * 16, (32 * e + 4 * t) * 1024 + w * 128);
      const u32 x2 = ld16(rx, (4 * c + g) * 2, w * 128);
      cfence();
#pragma unroll
      for (int t = 0; t < 8; ++t) *(v4u*)(lds + L_W + w * 8192 + t * 1024 + l * 16) = wf[t];
      v4u bw[8];
#pragma unroll
      for (int t = 0; t < 8; ++t) {
        typedef __attribute__((address_space(3))) v4s* lp;
        const char* b = lds + L_W + w * 8192 + (t >> 2) * 4096 + (32 * (t & 1) + 4 * (c >> 2) + g) * 64 + ((t >> 1) & 1) * 32 + (c & 3) * 8;
        const v2u lo = __builtin_bit_cast(v2u, __builtin_amdgcn_ds_read_tr16_b64_v4i16((lp)b));
        const v2u hi = __builtin_bit_cast(v2u, __builtin_amdgcn_ds_read_tr16_b64_v4i16((lp)(b + 1024)));
        bw[t] = v4u{lo.x, lo.y, hi.x, hi.y};
      }
      STAMP(6);
      const int j8 = (c & 7) >> 1;
      const v4u ME = {j8 == 0 ? hm : 0u, j8 == 1 ? hm : 0u, j8 == 2 ? hm : 0u, j8 == 3 ? hm : 0u};
      auto gu = [&](u32 hi, u32 lo, u32 dst) {
        const v4u A0 = ME & dppm<ROR8, 0xf, 0xc>(hi, lo), A1 = ME & dppm<ROR8, 0xf, 0x3>(hi, lo);
        float r[4];
#pragma unroll
        for (int k = 0; k < 4; ++k) {
          const v4f d = mfma32(A1, bw[2 * k + 1], mfma32(A0, bw[2 * k], Z4));
          r[k] = (d.x + d.y) + (d.z + d.w);
        }
        ldsf[dst / 4 + (w >> 2) * 256 + l * 4 + (w & 3)] = tred4(r[0], r[1], r[2], r[3]);
      };
      const u32 gsel = ((c >> 3) * 32 + rowa) * 16;
      auto gsum = [&](u32 src) {
        const v4f p = *(const v4f*)(lds + src + gsel), q = *(const v4f*)(lds + src + 1024 + gsel);
        const v4f s = p + q;
        return (s.x + s.y) + (s.z + s.w);
      };
      float cb1, S1, S2;
      {
        v4u y[1];
        const u32 vo[1] = {(u32)((g & 1) * 8192 + (g >> 1) * 2048 + c * 16)};
        gather<1, false>(y, ws, vo, WS_MR + 2048 + w * 256, TP);
        auto r = __builtin_amdgcn_permlane32_swap(asu(sw16add(asf(y[0].x), asf(y[0].y))), asu(sw16add(asf(y[0].z), asf(y[0].w))), false, false);
        const u32 cd = dup(1.f + rbf(asf(r[1])));
        cb1 = asf(cd & 0xffff0000u);
        gu(cd, 0u, L_G1);
        gu(dup(asf(r[0])), 0u, L_G2);
        bar();
        S1 = gsum(L_G1); S2 = gsum(L_G2);
      }
      STAMP(7);
      {
        slp<DLY_H4>();
        v4u y[4];
        const u32 v0 = g * 2048 + c * 16;
        const u32 vo[4] = {v0, v0 + 8192, v0 + 16384, v0 + 24576};
        gather<4, false>(y, ws, vo, WS_H4 + w * 256, TP);
        STAMP(8);
        const v4f s4 = (asf4(y[0]) + asf4(y[1])) + (asf4(y[2]) + asf4(y[3]));
        const float x1 = rbf(asf(x2 << 16) + rbf(tred4(s4.x, s4.y, s4.z, s4.w)));
        const float u = x1 * cb1;
        const u32 hi = dup(u);
        gu(hi, dup(u - asf(hi & 0xffff0000u)), L_G3);
        float r = sw32add(x1, x1 * x1);
        r = rowsum(sw16add(r, r));
        if ((l & 31) == 0) ldsf[L_SX / 4 + (l >> 5) * 8 + w] = r;
      }
      STAMP(9);
      bar();
      {
        float st = ldsf[L_SX / 4 + c];
        const float G = gsum(L_G3);
        st += dppf<QP(1, 0, 3, 2)>(st); st += dppf<QP(2, 3, 0, 1)>(st); st += dppf<HMIR>(st);
        const float mean = asf(__builtin_amdgcn_readlane(asu(st), 0)) * (1.f / 512.f);
        const float msq = asf(__builtin_amdgcn_readlane(asu(st), 8)) * (1.f / 512.f);
        const float var = rbf(fmaxf(msq - mean * mean, 0.f));
        const float den = rbf(__builtin_amdgcn_sqrtf(rbf(var + 1e-5f)));
        const u32 val = asu(rbf(fmaf(fmaf(-rbf(mean), S1, G), __builtin_amdgcn_rcpf(den), S2)));
        const float gt = asf(dppm<ROR8, 0xf, 0xc>(val, val)), uu = asf(dppm<ROR8, 0xf, 0x3>(val, val));
        const float z = gt * fmaf(gt * gt, 0.044715f, 1.f) * (-2.f * 0.7978845608028654f * 1.4426950408889634f);
        const float ge = rbf(gt * __builtin_amdgcn_rcpf(1.f + __builtin_amdgcn_exp2f(z)));
        const v4f acc = gemv4(MK, dup(ge * uu), wdn);
        const v4f o = {half1(acc.x), half1(acc.y), half1(acc.z), half1(acc.w)};
        if (c < 8 && (g & 1) == 0) st128(ws, enc4(o, TP), c * 32 + g * 8, WS_H5 + e * 2048 + w * 256);
      }
      STAMP(10);
    } else {
      if (i < 15) {
        const int m = xcd * 8 + i - 7, ks = m >> 5, cbk = m & 31;
        v4u wM[4];
#pragma unroll
        for (int t = 0; t < 4; ++t) wM[t] = ld128<AUX_NT>(rmod, rowl * 6144 + (c & 7) * 16, (256 * ks + 32 * w + 4 * t) * 6144 + 2048 + cbk * 128);
        const u32 cM = ld16(rc, rowa * 2, (256 * ks + 32 * w) * 2);
        half2(gemv4(MK, dup(silu_bf(cM)), wM), ldsf + L_RM / 4 + w * 64 + col2, c);
        STAMP(6);
        bar();
        if (w == 0 && l < 16) {
          const v4f* p = (const v4f*)(lds + L_RM) + l;
          const v4f s = ((p[0] + p[16]) + (p[32] + p[48])) + ((p[64] + p[80]) + (p[96] + p[112]));
          st128(ws, enc4(s, TP), l * 16, WS_MR + ks * 8192 + cbk * 256);
        }
        STAMP(7);
      }
      if (i >= 12 && w == 0) {
        const int r = xcd * 4 + i - 12;
        const int col = 4 * (c & 3) + g;
        const u32 x2 = ld16(rx, col * 2, r * 32);
        float gb, x1;
        {
          v4u y[1];
          const u32 vo[1] = {(u32)((g & 1) * 8192 + (c & 3) * 16)};
          gather<1, false>(y, ws, vo, WS_MR + 6144 + r * 64, TP);
          auto q = __builtin_amdgcn_permlane32_swap(asu(sw16add(asf(y[0].x), asf(y[0].y))), asu(sw16add(asf(y[0].z), asf(y[0].w))), false, false);
          gb = rbf(asf(q[0]));
        }
        const u32 v0 = (l >> 2) * 2048 + (l & 3) * 16;
        {
          slp<DLY_H4>();
          v4u y[1];
          const u32 vo[1] = {v0};
          gather<1, false>(y, ws, vo, WS_H4 + r * 64, TP);
          STAMP(8);
          float s = tred4(asf(y[0].x), asf(y[0].y), asf(y[0].z), asf(y[0].w));
          s += dppf<ROR8>(s); s += dppf<ROR4>(s);
          x1 = rbf(asf(x2 << 16) + rbf(s));
        }
        {
          slp<DLY_H5>();
          v4u y[4];
          const bool in = l < 32;
          const u32 vo[4] = {v0, v0 + 32768, v0 + 65536, in ? v0 + 98304 : v0};
          gather<4, false>(y, ws, vo, WS_H5 + r * 64, TP);
          STAMP(9);
          const float k3 = in ? 1.f : 0.f;
          const v4f s4 = (asf4(y[0]) + asf4(y[1])) + (asf4(y[2]) + asf4(y[3]) * k3);
          float s = tred4(s4.x, s4.y, s4.z, s4.w);
          s += dppf<ROR8>(s); s += dppf<ROR4>(s);
          const u32 o = dup(x1 + rbf(gb * rbf(s)));
          if (c < 4) out[r * 16 + col] = (u16)o;
          if (r == 0 && l == 0) st32(ws, T, 0, WS_EP);
        }
        STAMP(10);
      }
    }
  } else {
    v4u vr[4];
    asm volatile("s_waitcnt vmcnt(5)" ::: "memory");
    STAMP(1);
    vr[0] = ld128<AUX_NT>(rv, voV, sb0 + ((2 + rot) & 3) * 2048);
    vr[1] = ld128<AUX_NT>(rv, voV + 512, sb0 + ((2 + rot) & 3) * 2048);
    cfence();
    const u32 T = (u32)__builtin_amdgcn_readfirstlane(ep) + 1u, TP = T & TAGM;
    const int I = 2 * xcd + (i >> 3), J = i & 7;
    v4u wD[4];
    {
      if (T_Q > 0) { while (now() - t_start < T_Q) __builtin_amdgcn_s_sleep(1); }
      STAMP(2);
      v4u y[2];
      const u32 vo[2] = {(u32)(g * 32768 + c * 16), (u32)(g * 32768 + c * 16 + 256)};
      gather<2, true>(y, ws, vo, WS_H2 + xcd * 4096 + w * 512, T);
      cfence();
      vr[2] = ld128<AUX_NT>(rv, voV, sb0 + ((3 + rot) & 3) * 2048);
      vr[3] = ld128<AUX_NT>(rv, voV + 512, sb0 + ((3 + rot) & 3) * 2048);
      const rsrc_t rwo = mk(wo, 4096 * 512 * 2);
#pragma unroll
      for (int t = 0; t < 4; ++t) wD[t] = ld128<AUX_NT>(rwo, (16 * (c >> 3) + g) * 1024 + (c & 7) * 16, (256 * I + 32 * w + 4 * t) * 1024 + J * 128);
      cfence();
      STAMP(3);
      const float qv = tred4(asf(y[0].x), asf(y[0].z), asf(y[1].x), asf(y[1].z));
      *(u16*)(lds + L_Q + (64 * w + 2 * c + (g & 1) + 32 * (g >> 1)) * 2) = (u16)dup(qv);
    }
    bar();
    float sc[8];
    {
      const v4u q0 = *(const v4u*)(lds + L_Q + (c >> 2) * 256 + (2 * g + (c & 1)) * 16);
      const v4u q1 = *(const v4u*)(lds + L_Q + (c >> 2) * 256 + (2 * g + (c & 1)) * 16 + 128);
#pragma unroll
      for (int q = 0; q < 4; ++q) {
        const v4f d = mfma32(R[2 * q + 1], q1, mfma32(R[2 * q], q0, Z4));
        auto p0 = __builtin_amdgcn_permlane32_swap(asu(d.x), asu(d.y), false, false);
        auto p1 = __builtin_amdgcn_permlane32_swap(asu(d.z), asu(d.w), false, false);
        const float s0 = asf(p0[0]) + dppf<QP(1, 1, 1, 1)>(asf(p0[1])), s1 = asf(p1[0]) + dppf<QP(1, 1, 1, 1)>(asf(p1[1]));
        const u32 r1 = dppu<QP(0, 0, 0, 0)>(cvtpk(s0, s1));
        const u32 r2 = cvtpk(asf(r1 << 16) * 0.08838834764831845f, asf(r1 & 0xffff0000u) * 0.08838834764831845f);
        sc[2 * q] = asf(r2 << 16) * 1.4426950408889634f;
        sc[2 * q + 1] = asf(r2 & 0xffff0000u) * 1.4426950408889634f;
      }
    }
    float mw = fmaxf(fmaxf(fmaxf(sc[0], sc[1]), fmaxf(sc[2], sc[3])), fmaxf(fmaxf(sc[4], sc[5]), fmaxf(sc[6], sc[7])));
    mw = sw32max(sw16max(mw));
    float lw = 0.f;
#pragma unroll
    for (int k = 0; k < 8; ++k) { sc[k] = __builtin_amdgcn_exp2f(sc[k] - mw); lw += sc[k]; }
    lw = sw16add(lw, lw); lw = sw32add(lw, lw);
    if (l < 16 && (c & 3) == 0) { ldsf[L_ML / 4 + (c >> 2) * 8 + w] = mw; ldsf[L_ML / 4 + 32 + (c >> 2) * 8 + w] = lw; }
    STAMP(4);
    bar();
    v2u pa[8];
    {
      const v4f m0 = *(const v4f*)(ldsf + L_ML / 4 + (c >> 2) * 8), m1 = *(const v4f*)(ldsf + L_ML / 4 + (c >> 2) * 8 + 4);
      const float M = fmaxf(fmaxf(fmaxf(m0.x, m0.y), fmaxf(m0.z, m0.w)), fmaxf(fmaxf(m1.x, m1.y), fmaxf(m1.z, m1.w)));
      if (w == 7) {
        const v4f l0 = *(const v4f*)(ldsf + L_ML / 4 + 32 + (c >> 2) * 8), l1 = *(const v4f*)(ldsf + L_ML / 4 + 32 + (c >> 2) * 8 + 4);
        float L = l0.x * __builtin_amdgcn_exp2f(m0.x - M);
        L = fmaf(l0.y, __builtin_amdgcn_exp2f(m0.y - M), L); L = fmaf(l0.z, __builtin_amdgcn_exp2f(m0.z - M), L); L = fmaf(l0.w, __builtin_amdgcn_exp2f(m0.w - M), L);
        L = fmaf(l1.x, __builtin_amdgcn_exp2f(m1.x - M), L); L = fmaf(l1.y, __builtin_amdgcn_exp2f(m1.y - M), L); L = fmaf(l1.z, __builtin_amdgcn_exp2f(m1.z - M), L); L = fmaf(l1.w, __builtin_amdgcn_exp2f(m1.w - M), L);
        const v4u u = {asu(M), T, asu(L), T};
        if (l < 32 && (c & 3) == 0) st128(ws, u, (c >> 2) * 16 + g * 64, WS_ST + idx * 128);
      }
      const float cw = __builtin_amdgcn_exp2f(mw - M);
      const u32 MA0 = (c & 2) ? 0u : hm, MA1 = (c & 2) ? hm : 0u;
#pragma unroll
      for (int k = 0; k < 8; ++k) { const u32 d = dup(sc[k] * cw); pa[k] = v2u{d & MA0, d & MA1}; }
    }
    STAMP(5);
    {
      v4f a0 = Z4, a1 = Z4;
#pragma unroll
      for (int k = 0; k < 8; ++k) {
        const v4u v = k < 4 ? R[8 + k] : vr[k - 4];
        a0 = mfma16(pa[k], v2u{v.x, v.y}, a0);
        a1 = mfma16(pa[k], v2u{v.z, v.w}, a1);
      }
      v4f* p = (v4f*)(lds + L_O + w * 2048 + g * 512 + c * 32);
      p[0] = a0; p[1] = a1;
    }
    STAMP(6);
    bar();
    if (w < 2) {
      const v4f* p = (const v4f*)(lds + L_O) + tid;
      const v4f s = ((p[0] + p[128]) + (p[256] + p[384])) + ((p[512] + p[640]) + (p[768] + p[896]));
      st128(ws, enc4(s, TP), tid * 16, WS_H3 + idx * 2048);
    }
    STAMP(7);
    const int hl = 2 * (i >> 3) + (w >> 2);
    float f0, f1;
    {
      v4u y[2];
      const u32 vo[2] = {(u32)((l >> 3) * 128), (u32)((l >> 3) * 128 + 1024)};
      gather<2, true>(y, ws, vo, WS_ST + xcd * 2048 + hl * 16, T);
      float M = fmaxf(asf(y[0].x), asf(y[1].x));
      M = fmaxf(M, dppf<ROR8>(M)); M = sw32max(sw16max(M));
      const float e0 = __builtin_amdgcn_exp2f(asf(y[0].x) - M), e1 = __builtin_amdgcn_exp2f(asf(y[1].x) - M);
      float L = fmaf(e0, asf(y[0].z), e1 * asf(y[1].z));
      L += dppf<ROR8>(L); L = sw16add(L, L); L = sw32add(L, L);
      const float ri = __builtin_amdgcn_rcpf(L);
      f0 = e0 * ri; f1 = e1 * ri;
    }
    v4f ga = Z4;
    if (w == 0) {
      v4u y[2];
      const u32 vo[2] = {(u32)(c * 16), (u32)(c * 16 + 8192)};
      gather<2, false>(y, ws, vo, WS_MR + J * 256, TP);
      const v4f s = asf4(y[0]) + asf4(y[1]);
      ga = v4f{rbf(s.x), rbf(s.y), rbf(s.z), rbf(s.w)};
    }
    STAMP(8);
    {
      slp<DLY_H3>();
      v4u y[2];
      const u32 vo[2] = {(u32)((l >> 3) * 2048 + (l & 7) * 16), (u32)((l >> 3) * 2048 + (l & 7) * 16 + 16384)};
      gather<2, false>(y, ws, vo, WS_H3 + xcd * 32768 + hl * 512 + (w & 3) * 128, TP);
      STAMP(9);
      const v4f s = asf4(y[0]) * f0 + asf4(y[1]) * f1;
      const float v = tred4(s.x, s.y, s.z, s.w);
      half2(gemv4(MK, dup(v + dppf<ROR8>(v)), wD), ldsf + L_RD / 4 + w * 64 + col2, c);
    }
    bar();
    if (w == 0 && l < 16) {
      const v4f* p = (const v4f*)(lds + L_RD) + l;
      const v4f s = ((p[0] + p[16]) + (p[32] + p[48])) + ((p[64] + p[80]) + (p[96] + p[112]));
      st128(ws, enc4(s * ga, TP), l * 16, WS_H4 + I * 2048 + J * 256);
    }
    STAMP(10);
    asm volatile("" :: "v"(pf));
  }
}

extern "C" void kernel_launch(void* const* d_in, const int* in_sizes, int n_in, void* d_out, int out_size, void* d_ws, size_t ws_size,
                              hipStream_t stream) {
  (void)in_sizes; (void)n_in; (void)out_size; (void)ws_size;
  fused<<<256, 512, 0, stream>>>((const u16*)d_in[0], (const u16*)d_in[1], (const u16*)d_in[2], (const u16*)d_in[3], (const u16*)d_in[4],
                                 (const u16*)d_in[6], (const u16*)d_in[7], (const u16*)d_in[8], (const u16*)d_in[9], (const u16*)d_in[10],
                                 (u16*)d_out, (char*)d_ws);
}
```

```cpp
#define T_TCH 400
#define MR_EARLY 0
#define QW0 1
#define NO_PF 1
#define DN_LAST 0
#define GU_PF 1
#define AUX_PF 0
#define AUX_GU 0
#define STEP 1
#define WO_POS 3
#define D_H3 22
#define TB 3
#define TOFF 0
#define T_H1 195
#define T_Q 415
#define T_MR 335
#define T_PG 450
#define T_DN 570
#define T_GU 585
#define T_MG 600
#define T_H4 774
#define T_H5 918
#define T_TK 228
#define SLOWP 2
#define PFW 0
#define DNBR 1
#define DNPF 4
#define WOPF 1
#define GT 1
#define T_PW 450
#define T_H3 650
#define SSP 0
#define RING 0
#define PDG 55
#define T_H4O 801
#include <hip/hip_runtime.h>
#include <stdint.h>

typedef unsigned int u32;
typedef unsigned short u16;
typedef u32 v4u __attribute__((ext_vector_type(4)));
typedef u32 v2u __attribute__((ext_vector_type(2)));
typedef float v4f __attribute__((ext_vector_type(4)));
typedef float v2f __attribute__((ext_vector_type(2)));
typedef __bf16 v8bf __attribute__((ext_vector_type(8)));
typedef __bf16 v2bf __attribute__((ext_vector_type(2)));
typedef short v4s __attribute__((ext_vector_type(4)));
typedef __amdgpu_buffer_rsrc_t rsrc_t;
#define DI __device__ __forceinline__

#define PROF 0

constexpr u32 WS_EP = 0;
constexpr u32 WS_H1 = 4096;
constexpr u32 WS_H2 = WS_H1 + 65536;
constexpr u32 WS_ST = WS_H2 + 131072;
constexpr u32 WS_MR = WS_ST + 16384;
constexpr u32 WS_H4 = WS_MR + 16384;
constexpr u32 WS_H3 = WS_H4 + 32768;
constexpr u32 WS_H5 = WS_H3 + 262144;
constexpr u32 WS_CS = WS_H5 + 56 * 2048 - WS_ST;
constexpr u32 WS_END = WS_ST + 4 * WS_CS;
constexpr u32 WS_T0 = 2048;
constexpr u32 WS_PROF = 3u << 20;

constexpr int AUX_NT = 2, AUX_SC1 = 16;
#define AUX_PL 16
#define AUX_ST 16
constexpr u32 SPIN_MAX = 1u << 16;
constexpr u32 OOB = 0x80000000u;
constexpr u32 TAGM = 1023u;

#define AUX_DN 2
#define DLY_RE 0
#define CODE_PF 5
#define DSP 0
#define QH 8
#define PFV 0
#define T_PF 0
#define AUX_V 2
#define DIAG_V 0
#define T_V 0
#define CPF_LATE 1
#define WK1 4
#define WK2 4
#define WSW 8
#define WF 8
#define DN_EARLY 0
#define KFLIP 0
#define PF_FIRST 1
#define KVD 0
#define QWP 0
#define PFD 3
#define DPF 20
#define PF_LATE 1
#define WK3 0
#define MR_FIRST 1
#define MG_EARLY 1
#define D_H5 0
#define KV1 1
#define PV 0
#define T_PV 210
#define PVAUX " nt"
#define T_WO 400
#define PACE 0
#define KT0 55
#define KD1 45
#define KD2 32
#define KD3 40
#define WKW 4
#define HT1 3
#define HT2 1
#define VF 0
#define VFP 0
#define TOFQ (TB == 2 ? 73 : TOFF)
#define TLBT 0
#define FRE 1
#define GTW 600
#define T_H1F 228
#define OTB 0
#define UT 0
#define AUX_WO 2
#define DH3C 12
#define STL 1
#define GUP 0
#define HALF 0
#define DMA 1
#define SPRE 0
#define N3 0
#define D3 25
#define N4 0
#define D4 28
#define N5 0
#define D5 14
#define CSX WS_CS
#define PT 0
#define PTB WS_H2
#define PTD 0
#define PTM 0
#define PW 0
#define T_PWK 0
#define PT1 0
#define PDL 0
#define S3 0
#define DR 5
#define C5 (RING ? 2 : N5)
#define KEEP4 1
#define LP ".L"
#define SPAD(id) do { __builtin_amdgcn_sched_barrier(0); asm volatile(LP "sp%0:\n\ts_cbranch_execz " LP "sn%0" :: "n"(id) : "memory"); __builtin_amdgcn_sched_barrier(0); } while (0)

DI rsrc_t mk(const void* p, u32 bytes) { return __builtin_amdgcn_make_buffer_rsrc((void*)p, 0, bytes, 0x00020000); }
template <int AUX> DI v4u ld128(rsrc_t r, u32 vo, u32 so) { return __builtin_amdgcn_raw_buffer_load_b128(r, vo, so, AUX); }
template <int AUX> DI u32 ld32(rsrc_t r, u32 vo, u32 so) { return __builtin_amdgcn_raw_buffer_load_b32(r, vo, so, AUX); }
DI u32 ld16(rsrc_t r, u32 vo, u32 so) { return (u32)(u16)__builtin_amdgcn_raw_buffer_load_b16(r, vo, so, 0); }
DI void st32(rsrc_t r, u32 v, u32 vo, u32 so) { __builtin_amdgcn_raw_buffer_store_b32(v, r, vo, so, AUX_ST); }
DI void st64(rsrc_t r, v2u v, u32 vo, u32 so) { __builtin_amdgcn_raw_buffer_store_b64(v, r, vo, so, AUX_ST); }
DI void st16(rsrc_t r, u32 v, u32 vo, u32 so) { __builtin_amdgcn_raw_buffer_store_b16((u16)v, r, vo, so, 0); }
DI void st128(rsrc_t r, v4u v, u32 vo, u32 so) { __builtin_amdgcn_raw_buffer_store_b128(v, r, vo, so, AUX_ST); }
DI float asf(u32 x) { return __uint_as_float(x); }
DI u32 asu(float x) { return __float_as_uint(x); }
DI u32 cvtpk(float lo, float hi) { v2f v = {lo, hi}; return __builtin_bit_cast(u32, __builtin_convertvector(v, v2bf)); }
DI u32 dup(float x) { return cvtpk(x, x); }
DI float rbf(float x) { return asf(cvtpk(0.f, x)); }
DI float fmx(float a, float b) { return __builtin_elementwise_maximum(a, b); }
#define MAXD(x, ctl) asm("s_nop 1\n\tv_max_f32_dpp %0, %1, %1 " ctl " row_mask:0xf bank_mask:0xf" : "=v"(x) : "v"(x))
#define Q1032 "quad_perm:[1,0,3,2]"
#define Q2301 "quad_perm:[2,3,0,1]"
template <int C> DI u32 dppu(u32 x) { return (u32)__builtin_amdgcn_update_dpp(0, (int)x, C, 0xf, 0xf, true); }
template <int C> DI float dppf(float x) { return asf(dppu<C>(asu(x))); }
template <int C, int RM, int BM> DI u32 dppm(u32 old, u32 x) { return (u32)__builtin_amdgcn_update_dpp((int)old, (int)x, C, RM, BM, false); }
constexpr int ROR8 = 0x128, ROR4 = 0x124, HMIR = 0x141;
constexpr int QP(int a, int b, int c, int d) { return a | (b << 2) | (c << 4) | (d << 6); }
DI float sw32add(float a, float b) { auto r = __builtin_amdgcn_permlane32_swap(asu(a), asu(b), false, false); return asf(r[0]) + asf(r[1]); }
DI float sw16add(float a, float b) { auto r = __builtin_amdgcn_permlane16_swap(asu(a), asu(b), false, false); return asf(r[0]) + asf(r[1]); }
DI float sw32max(float a) { auto r = __builtin_amdgcn_permlane32_swap(asu(a), asu(a), false, false); return fmx(asf(r[0]), asf(r[1])); }
DI float sw16max(float a) { auto r = __builtin_amdgcn_permlane16_swap(asu(a), asu(a), false, false); return fmx(asf(r[0]), asf(r[1])); }
DI float tred4(float x, float y, float z, float w) { return sw16add(sw32add(x, z), sw32add(y, w)); }
DI float rowsum(float v) { v += dppf<ROR8>(v); v += dppf<ROR4>(v); v += dppf<QP(2, 3, 0, 1)>(v); v += dppf<QP(1, 0, 3, 2)>(v); return v; }
DI float wavesum(float v) { v = rowsum(v); v = sw16add(v, v); return sw32add(v, v); }
DI u32 tagm() { u32 m = TAGM; asm volatile("" : "+v"(m)); return m; }
DI u32 enc(float v, u32 T, u32 m) { return __builtin_amdgcn_bitop3_b32(asu(v), T, m, 0xD8); }
DI v4f asf4(v4u v) { return __builtin_bit_cast(v4f, v); }
DI void cfence() { asm volatile("" ::: "memory"); }
DI void bar() { asm volatile("s_waitcnt lgkmcnt(0)\n\ts_barrier" ::: "memory"); }
DI v4f mfma32(v4u a, v4u b, v4f c) { return __builtin_amdgcn_mfma_f32_16x16x32_bf16(__builtin_bit_cast(v8bf, a), __builtin_bit_cast(v8bf, b), c, 0, 0, 0); }
DI v4f mfma16(v2u a, v2u b, v4f c) { return __builtin_amdgcn_mfma_f32_16x16x16bf16_1k(__builtin_bit_cast(v4s, a), __builtin_bit_cast(v4s, b), c, 0, 0, 0); }
DI float silu_bf(u32 h) {
  const float v = asf(h << 16), d = 1.f + __builtin_amdgcn_exp2f(fminf(-1.4426950408889634f * v, 126.f)), r = __builtin_amdgcn_rcpf(d), q = v * r;
  return fmaf(fmaf(-d, q, v), r, q);
}
DI float dot2(u32 a, u32 b, float c) { return __builtin_amdgcn_fdot2_f32_bf16(__builtin_bit_cast(v2bf, a), __builtin_bit_cast(v2bf, b), c, false); }
template <int N> DI void slp() { if (N > 0) __builtin_amdgcn_s_sleep(N); }
DI u32 now() { return (u32)__builtin_amdgcn_s_memrealtime(); }
DI v4f gemv4(v4u MK, u32 act, const v4u (&wt)[4]) {
  const v4f Z = {0.f, 0.f, 0.f, 0.f};
  v4f a0 = mfma32(MK & dppu<QP(0, 0, 0, 0)>(act), wt[0], Z);
  v4f a1 = mfma32(MK & dppu<QP(1, 1, 1, 1)>(act), wt[1], Z);
  a0 = mfma32(MK & dppu<QP(2, 2, 2, 2)>(act), wt[2], a0);
  a1 = mfma32(MK & dppu<QP(3, 3, 3, 3)>(act), wt[3], a1);
  return a0 + a1;
}
DI void half2(v4f acc, float* p) {
  auto p0 = __builtin_amdgcn_permlane16_swap(asu(acc.x), asu(acc.y), false, false);
  auto p1 = __builtin_amdgcn_permlane16_swap(asu(acc.z), asu(acc.w), false, false);
  const float s0 = asf(p0[0]) + dppf<ROR8>(asf(p0[1])), s1 = asf(p1[0]) + dppf<ROR8>(asf(p1[1]));
  p[0] = s0; p[2] = s1;
}
DI float half1(float x) { auto p = __builtin_amdgcn_permlane16_swap(asu(x), asu(x), false, false); return x + dppf<ROR8>(asf(p[1])); }

DI u32 df2(u32 a, u32 b, u32 T) { return __builtin_amdgcn_bitop3_b32(a, b, T, 0x7E); }
template <bool WIDE> DI u32 diff(v4u y, u32 T) {
  if (WIDE) return df2(y.y, y.w, T);
  return __builtin_amdgcn_bitop3_b32(df2(y.x, y.y, T), df2(y.z, y.w, T), TAGM, 0xA8);
}
template <int N, bool WIDE> DI bool anystale(const v4u (&y)[N], u32 T) {
  u32 any = 0;
#pragma unroll
  for (int k = 0; k < N; ++k) any |= diff<WIDE>(y[k], T);
  asm volatile("" : "+v"(any));
  return __builtin_amdgcn_ballot_w64(any != 0) != 0;
}
template <int N, bool WIDE, int ID> DI void settle(v4u (&y)[N], rsrc_t ws, const u32 (&vo)[N], u32 so, u32 T) {
  if (__builtin_expect(anystale<N, WIDE>(y, T), 0)) {
    SPAD(ID);
    u32 spin = 0;
#pragma unroll 1
    do {
      slp<DLY_RE>();
      cfence();
      v4u n[N];
      bool bad[N];
#pragma unroll
      for (int k = 0; k < N; ++k) { bad[k] = diff<WIDE>(y[k], T) != 0; n[k] = ld128<AUX_PL>(ws, bad[k] ? vo[k] : OOB, so); }
#pragma unroll
      for (int k = 0; k < N; ++k) y[k] = bad[k] ? n[k] : y[k];
    } while (anystale<N, WIDE>(y, T) && ++spin < SPIN_MAX);
  }
  if (!WIDE) {
#pragma unroll
    for (int k = 0; k < N; ++k) y[k] ^= T;
  }
}
template <int N, bool WIDE, int ID> DI void gather(v4u (&y)[N], rsrc_t ws, const u32 (&vo)[N], u32 so, u32 T) {
  cfence();
#pragma unroll
  for (int k = 0; k < N; ++k) y[k] = ld128<AUX_PL>(ws, vo[k], so);
  settle<N, WIDE, ID>(y, ws, vo, so, T);
}

DI u32 or4(v4u y) { return (y.x | y.y) | (y.z | y.w); }
DI v4u untag(v4u y, u32 T) { return y ^ T; }
#define GD
#define GA
#define GS(k, v) do {} while (0)
template <int N, int ID, int NS, class F, class M, class A> DI u32 gath(rsrc_t ws, const u32 (&vo)[N], u32 so, u32 T, u32 Tl, F f, M mid, A at, v4u (&x)[NS + 1][N] GD) {
  v4u y[N];
  GS(12, now()); GS(13, 0);
  cfence();
#pragma unroll
  for (int k = 0; k < N; ++k) y[k] = ld128<AUX_PL>(ws, vo[k], so);
  mid();
#pragma unroll
  for (int q = 0; q < NS; ++q) {
    at(q + 1);
    cfence();
#pragma unroll
    for (int k = 0; k < N; ++k) x[q][k] = ld128<AUX_PL>(ws, vo[k], so + (q + 1) * CSX);
    cfence();
  }
  GS(11, now());
  v4f s;
  u32 any = 0;
#pragma unroll
  for (int k = 0; k < N; ++k) {
    y[k] = untag(y[k], k == N - 1 ? Tl : T);
    asm volatile("" : "+v"(y[k]));
    s = k ? s + asf4(y[k]) : asf4(y[k]);
    if (k < N - 1) { any = (any | y[k].x | y[k].y) | y[k].z | y[k].w; if (k) asm volatile("" : "+v"(any), "+v"(s)); else asm volatile("" : "+v"(any)); }
  }
  any = ((any | y[N - 1].x | y[N - 1].y) | (y[N - 1].z | y[N - 1].w)) & TAGM;
  u32 r = f(s);
  asm volatile("" : "+v"(r));
  if (__builtin_expect(__builtin_amdgcn_ballot_w64(any != 0) != 0, 0)) {
    SPAD(ID);
    GS(13, now());
    bool more = true;
#pragma unroll
    for (int q = 0; q < NS; ++q) {
      if (more) {
        cfence();
        GS(15, ts[0] + 100 * (q + 1));
        any = 0;
#pragma unroll
        for (int k = 0; k < N; ++k) { y[k] = (or4(y[k]) & TAGM) != 0 ? untag(x[q][k], k == N - 1 ? Tl : T) : y[k]; any |= or4(y[k]); }
        more = __builtin_amdgcn_ballot_w64((any & TAGM) != 0) != 0;
      }
    }
    u32 spin = 0;
    if (more) {
#pragma unroll 1
      do {
        cfence();
        GS(15, ts[0] + 100 * (spin + 1 + NS));
        v4u n[N];
        bool bad[N];
#pragma unroll
        for (int k = 0; k < N; ++k) { bad[k] = (or4(y[k]) & TAGM) != 0; n[k] = ld128<AUX_PL>(ws, bad[k] ? vo[k] : OOB, so); }
        any = 0;
#pragma unroll
        for (int k = 0; k < N; ++k) { y[k] = bad[k] ? untag(n[k], k == N - 1 ? Tl : T) : y[k]; any |= or4(y[k]); }
      } while (__builtin_amdgcn_ballot_w64((any & TAGM) != 0) != 0 && ++spin < SPIN_MAX);
    }
#pragma unroll
    for (int k = 0; k < N; ++k) s = k ? s + asf4(y[k]) : asf4(y[k]);
    r = f(s);
  }
  return r;
}
template <int N, int ID, class F, class E> DI void ring(rsrc_t ws, const u32 (&vo)[N], u32 so, u32 T, u32 Tl, F f, E fin GD) {
  v4u y[N];
  GS(12, now()); GS(13, 0);
  cfence();
#pragma unroll
  for (int k = 0; k < N; ++k) y[k] = ld128<AUX_PL>(ws, vo[k], so);
  v4f s;
  u32 any = 0;
#pragma unroll
  for (int k = 0; k < N; ++k) {
    y[k] = untag(y[k], k == N - 1 ? Tl : T);
    asm volatile("" : "+v"(y[k]));
    s = k ? s + asf4(y[k]) : asf4(y[k]);
    if (k < N - 1) { any = (any | y[k].x | y[k].y) | y[k].z | y[k].w; if (k) asm volatile("" : "+v"(any), "+v"(s)); else asm volatile("" : "+v"(any)); }
  }
  any = ((any | y[N - 1].x | y[N - 1].y) | (y[N - 1].z | y[N - 1].w)) & TAGM;
  u32 r = f(s);
  asm volatile("" : "+v"(r));
  if (__builtin_expect(__builtin_amdgcn_ballot_w64(any != 0) != 0, 0)) {
    SPAD(ID);
    GS(13, now());
    static_assert(N == 4, "");
    v4u x[12];
    v2f s01, s23;
    u32 cnt, tv;
#define RASK(b, so) "buffer_load_dwordx4 v[" #b "+0:" #b "+3], %16, %20, " so " offen sc1\n\tbuffer_load_dwordx4 v[" #b "+4:" #b "+7], %17, %20, " so " offen sc1\n\t" \
                    "buffer_load_dwordx4 v[" #b "+8:" #b "+11], %18, %20, " so " offen sc1\n\tbuffer_load_dwordx4 v[" #b "+12:" #b "+15], %19, %20, " so " offen sc1\n\t"
#define RX(b, i, t) "v_xor_b32 v[" #b "+" #i "], " t ", v[" #b "+" #i "]\n\t"
#define RO(b, i, k) "v_or3_b32 %15, %15, v[" #b "+" #i "], v[" #b "+" #k "]\n\t"
#define RCHK(b, out) "s_waitcnt vmcnt(8)\n\t" RX(b, 0, "%24") RX(b, 1, "%24") RX(b, 2, "%24") RX(b, 3, "%24") RX(b, 4, "%24") RX(b, 5, "%24") RX(b, 6, "%24") RX(b, 7, "%24") \
                     RX(b, 8, "%24") RX(b, 9, "%24") RX(b, 10, "%24") RX(b, 11, "%24") RX(b, 12, "%25") RX(b, 13, "%25") RX(b, 14, "%25") RX(b, 15, "%25") \
                     "v_or_b32 %15, v[" #b "+0], v[" #b "+1]\n\t" RO(b, 2, 3) RO(b, 4, 5) RO(b, 6, 7) RO(b, 8, 9) RO(b, 10, 11) RO(b, 12, 13) RO(b, 14, 15) \
                     "v_and_b32 %15, %26, %15\n\tv_cmp_ne_u32 vcc, 0, %15\n\ts_nop 4\n\ts_cbranch_vccz " out "\n\t"
#define RSUM(b) "v_pk_add_f32 %12, v[" #b "+0:" #b "+1], v[" #b "+4:" #b "+5]\n\tv_pk_add_f32 %13, v[" #b "+2:" #b "+3], v[" #b "+6:" #b "+7]\n\t" \
                "v_pk_add_f32 %12, %12, v[" #b "+8:" #b "+9]\n\tv_pk_add_f32 %13, %13, v[" #b "+10:" #b "+11]\n\t" \
                "v_pk_add_f32 %12, %12, v[" #b "+12:" #b "+13]\n\tv_pk_add_f32 %13, %13, v[" #b "+14:" #b "+15]\n\t"
    asm volatile(RASK(96, "%22") "s_sleep %27\n\t" RASK(112, "%23") "s_sleep %27\n\t" RASK(128, "%21") "s_movk_i32 %14, 0x7fff\n"
                 "1:\n\t" RCHK(96, "2f") RASK(96, "%22") RCHK(112, "3f") RASK(112, "%23") RCHK(128, "4f") RASK(128, "%21")
                 "s_sub_u32 %14, %14, 1\n\ts_cbranch_scc0 1b\n\ts_waitcnt vmcnt(0)\n"
                 "2:\n\t" RSUM(96) "s_branch 5f\n3:\n\t" RSUM(112) "s_branch 5f\n4:\n\t" RSUM(128) "\n5:"
                 : "=&{v[96:99]}"(x[0]), "=&{v[100:103]}"(x[1]), "=&{v[104:107]}"(x[2]), "=&{v[108:111]}"(x[3]),
                   "=&{v[112:115]}"(x[4]), "=&{v[116:119]}"(x[5]), "=&{v[120:123]}"(x[6]), "=&{v[124:127]}"(x[7]),
                   "=&{v[128:131]}"(x[8]), "=&{v[132:135]}"(x[9]), "=&{v[136:139]}"(x[10]), "=&{v[140:143]}"(x[11]),
                   "=&v"(s01), "=&v"(s23), "=&s"(cnt), "=&v"(tv)
                 : "v"(vo[0]), "v"(vo[1]), "v"(vo[2]), "v"(vo[3]), "s"(ws), "s"(so), "s"(so + WS_CS), "s"(so + 2 * WS_CS), "s"(T), "v"(Tl), "n"(TAGM), "n"(DR)
                 : "vcc", "scc", "memory");
    fin(f(v4f{s01.x, s01.y, s23.x, s23.y}));
    asm volatile("s_waitcnt vmcnt(0)" :: "{v[96:99]}"(x[0]), "{v[100:103]}"(x[1]), "{v[104:107]}"(x[2]), "{v[108:111]}"(x[3]),
                 "{v[112:115]}"(x[4]), "{v[116:119]}"(x[5]), "{v[120:123]}"(x[6]), "{v[124:127]}"(x[7]),
                 "{v[128:131]}"(x[8]), "{v[132:135]}"(x[9]), "{v[136:139]}"(x[10]), "{v[140:143]}"(x[11]) : "memory");
  } else fin(r);
}
template <int N, int NS> DI void keep(v4u (&x)[NS + 1][N]) {
#pragma unroll
  for (int q = 0; q < NS; ++q)
#pragma unroll
    for (int k = 0; k < N; ++k) asm volatile("" :: "v"(x[q][k]));
}

#define PADS 1
#define PAD(n, next) asm volatile(LP "pad" #n ":\n\ts_cbranch_execz " LP next)
#define PADV(n, next, v) asm volatile(LP "pad" #n ":\n\ts_cbranch_execz " LP next : "+v"(v))
#define PAGE(n) asm volatile("s_branch 1f\n\t.subsection " #n "\n\t.p2align 12\n1:")
#define SECT(n) asm volatile("s_branch 1f\n\t.subsection " #n "\n1:")
#define HOP(k, n) "s_cmp_eq_u32 %1, " #k "\n\ts_cbranch_scc1 " LP "pad" #n "\n\t"
#define TOUCH(ret, hops) do { uint64_t sv_; asm volatile("s_mov_b64 %0, exec\n\ts_mov_b64 exec, 0\n\t" hops LP ret ":\n\ts_mov_b64 exec, %0" : "=&s"(sv_) : "s"(w) : "scc"); } while (0)
#define STAMP(k) do {} while (0)

#define CHAIN(n, ret) do { uint64_t sv_; asm volatile("s_mov_b64 %0, exec\n\ts_mov_b64 exec, 0\n\ts_branch " LP "pad" #n "\n" LP ret ":\n\ts_mov_b64 exec, %0" : "=&s"(sv_)); } while (0)
#define RW "rete"
#define RF(x) "rete"
#define SP(n, x) "sp" #n
constexpr int ROR12 = 0x12C;
#define PROB(x, p) __builtin_expect_with_probability(!!(x), 1, p)
#define PT0() do {} while (0)
#define STAMPK() do {} while (0)
#define AI __attribute__((always_inline))
#define SB() __builtin_amdgcn_sched_barrier(0)
#define END() do { asm volatile("s_endpgm\n\t.subsection 0" ::: "memory"); return; } while (0)
#define ENDL(n) do { asm volatile("s_endpgm\n\t.subsection " #n ::: "memory"); return; } while (0)
#define POUT() do {} while (0)

constexpr u32 L_RA = 0, L_RB = 4096, L_RM = 12288;
constexpr u32 L_G1 = 0, L_G2 = 2048, L_G3 = 12288, L_SX = 14336, L_W = 17408;
constexpr u32 L_Q = 0, L_ML = 1024, L_TK = 1216, L_LS = 1280, L_RD = 4096, L_O = 16384;
constexpr u32 L_TR = 16384;
constexpr u32 LDS_BYTES = 84 * 1024;

#define ARGS const u16* __restrict__ xin, const u16* __restrict__ kc, const u16* __restrict__ vc, const u16* __restrict__ wq, \
             const u16* __restrict__ cin, const u16* __restrict__ wmod, char* wsp, u16* __restrict__ out, \
             const u16* __restrict__ wo, const u16* __restrict__ wg, const u16* __restrict__ wu, const u16* __restrict__ wd
#define ROLE(name) extern "C" __global__ void __launch_bounds__(768) __attribute__((amdgpu_num_sgpr(88))) name(const void* unused)
typedef const __attribute__((address_space(4))) uint64_t* kargs_t;
enum { A_X, A_K, A_V, A_WQ, A_C, A_MOD, A_WS, A_OUT, A_WO, A_WG, A_WU, A_WD };
#define ARG(k) ((char*)ka[k])
typedef u32 v8u __attribute__((ext_vector_type(8)));
#define PINIT() do {} while (0)

extern "C" __global__ void __launch_bounds__(768) __attribute__((aligned(4096))) fused(ARGS) {
  __shared__ __attribute__((aligned(16))) char lds[LDS_BYTES];
  asm volatile("s_load_dword s90, s[0:1], 0x0\n\ts_load_dword s91, s[0:1], 0x40\n\t"
               "v_readfirstlane_b32 s3, %1\n\ts_cmpk_gt_u32 s3, 511\n\ts_cbranch_scc1 1f\n\t"
               "s_cmpk_lt_u32 %0, 128\n\ts_cbranch_scc1 role_qa\n\ts_branch role_kv\n"
               "1:\n\ts_cmpk_lt_u32 %0, 128\n\ts_cbranch_scc1 2f\n\t"
               "s_getpc_b64 s[4:5]\n\ts_andn2_b64 s[4:5], s[4:5], 0xfff\n\tv_and_b32 v142, 3, %1\n\tv_lshlrev_b32 v142, 7, v142\n\t"
               "s_bfe_u32 s6, %0, 0x40003\n\ts_lshl_b32 s6, s6, 11\n\ts_lshl_b32 s3, s3, 3\n\ts_and_b32 s3, s3, 0x600\n\ts_add_u32 s6, s6, s3\n\t"
               "v_add_u32 v142, s6, v142\n\tv_min_u32 v142, " LP "end-fused-4, v142\n\t"
               "global_load_dword v143, v142, s[4:5] nt\n\t"
               "s_waitcnt lgkmcnt(0)\n\ts_memrealtime s[92:93]\n\t"
               "s_branch role_h\n"
               "2:\n\t"
               "s_mov_b64 exec, 0\n\t"
               "s_cmpk_lt_u32 s3, 576\n\ts_cbranch_scc1 " LP "pad20\n\t"
               "s_cmpk_lt_u32 s3, 640\n\ts_cbranch_scc1 " LP "pad21\n\t"
               "s_cmpk_lt_u32 s3, 704\n\ts_cbranch_scc1 " LP "pad22\n\t"
               "s_branch " LP "gt1\n\t.set " LP "sn1, " LP "retj\n\t"
               ".set " LP "sn2, " LP "rete\n\t.set " LP "sn3, " LP "rete\n\t.set " LP "sn4, " LP "rete\n\t"
               ".set " LP "sn5, " LP "rete\n\t.set " LP "sn6, " LP "rete\n\t.set " LP "sn7, " LP "reth\n\t.set " LP "sn8, " LP "reth\n"
               LP "reti:\n\ts_barrier\n" LP "retj:\n\t"
               "s_cmp_lt_u32 %0, 64\n\ts_cbranch_scc1 3f\n\t"
               "s_cmpk_lt_u32 s3, 576\n\ts_cbranch_scc1 " LP "pad1\n\t"
               "s_cmpk_lt_u32 s3, 640\n\ts_cbranch_scc1 " LP "pad2\n\t"
               "s_cmpk_lt_u32 s3, 704\n\ts_cbranch_scc1 " LP "pad3\n\t"
               "s_branch " LP "pad4\n"
               "3:\n\ts_cmpk_lt_u32 s3, 576\n\ts_cbranch_scc1 " LP "pad5\n\t"
               "s_cmpk_lt_u32 s3, 640\n\ts_cbranch_scc1 " LP "pad8\n\t"
               "s_cmpk_lt_u32 s3, 704\n\ts_cbranch_scc1 " LP "pad6\n\t"
               "s_branch " LP "pad7\n"
               LP "rete:\n\ts_waitcnt vmcnt(0)\n\ts_endpgm"
               :: "s"(blockIdx.x), "v"(threadIdx.x), "v"((u32)(size_t)lds)
               : "scc", "memory", "vcc", "s3", "s4", "s5", "s6", "s7", "s8", "s9", "s10", "s11", "s12", "s13", "s14", "s15", "s16", "s17", "s18", "s19", "s20", "s21", "s22", "s23", "m0", "s95", "v142", "v143", "v144", "v145", "v146", "v147", "v148", "v149", "v150", "v151", "v152", "v153", "v167");
}

#define PROLOG() \
  __shared__ __attribute__((aligned(16))) char lds[LDS_BYTES]; \
  PINIT(); \
  int tid = threadIdx.x, blk = blockIdx.x; \
  kargs_t ka = (kargs_t)__builtin_amdgcn_kernarg_segment_ptr(); \
  v4u R[16]; (void)R
#define FENCE() do { SB(); asm volatile("" : "+v"(tid), "+s"(blk), "+s"(ka)); SB(); } while (0)
#define IDS(TO) \
  constexpr int toff = TO; \
  const int w = __builtin_amdgcn_readfirstlane(tid >> 6); \
  const int l = tid & 63, g = l >> 4, c = l & 15; \
  const int xcd = blk & 7, j = blk >> 3; \
  const int a = (l >> 2) & 1; \
  const u32 vox = l * 16, kvo = (xcd * 65536 + 61440) * 256; \
  u32 t_start; \
  auto until = [&](int t) AI { if (t > 0 && t > toff) { while (now() - t_start < (u32)(t - toff)) __builtin_amdgcn_s_sleep(1); } }; \
  char* const wsp = ARG(A_WS); \
  const rsrc_t ws = mk(wsp, WS_END); \
  const rsrc_t rx = mk(ARG(A_X), 1024), rc = mk(ARG(A_C), 1024), rmod = mk(ARG(A_MOD), 512 * 3072 * 2); \
  const u32 rowa = 16 * a + 4 * (c & 3) + g;            \
  const u32 rowl = 16 * (c >> 3) + g;                   \
  const v4f Z4 = {0.f, 0.f, 0.f, 0.f}; \
  float* ldsf = (float*)lds; \
  asm volatile("" :: "s"((u32)(size_t)lds));            \
  (void)vox; (void)kvo; (void)until; (void)ws; (void)rx; (void)rc; (void)rmod; (void)rowa; (void)rowl; (void)Z4; (void)ldsf; (void)j
#define EPOCH() u32 ep_; asm volatile("s_load_dword %0, %1, 0x0" : "=s"(ep_) : "s"(wsp))
#define COMMON() \
  u32 ep, t0_; \
  asm volatile("s_waitcnt lgkmcnt(0)\n\ts_mov_b32 %0, %2\n\ts_mov_b32 %1, s92" : "=&s"(ep), "=&s"(t0_) : "s"(ep_)); \
  t_start = t0_; PT0(); \
  const int jj = (c & 3) + 4 * (c >> 3); \
  const u32 hm = (c & 1) ? 0xffff0000u : 0x0000ffffu; \
  const v4u MK = {(jj >> 1) == 0 ? hm : 0u, (jj >> 1) == 1 ? hm : 0u, (jj >> 1) == 2 ? hm : 0u, (jj >> 1) == 3 ? hm : 0u}; \
  const u32 T = ep + 1u, TP = T & TAGM; \
  const int col2 = 8 * c + (g & 1) + 4 * (g >> 1); (void)col2; (void)MK; (void)TP

template <int N> DI void vmw() { asm volatile("s_waitcnt vmcnt(%0)" :: "n"(N) : "memory"); }

ROLE(role_kv) {
  PROLOG();
  {
    u32 soa, voa, vob;
    char *kp = ARG(A_K), *vp = ARG(A_V);
    {
      const u32 t0 = __builtin_amdgcn_readfirstlane(tid);
      soa = (((blk & 7) ^ KFLIP) << 24) + (61440 * 256 - 16 * 8192) + ((t0 >> 8) << 19) + ((blk >> 3) << 13) + ((t0 & 192) << 5);
      voa = (tid & 14) * 128 + (tid & 48) * 2 + (tid & 1) * 16;
      vob = (tid & 48) * 32 + (tid & 15) * 16;
      if (VF) asm volatile("" : "+s"(kp), "+s"(vp) : "v"(voa), "s"(soa), "v"(vob));
      else asm volatile("" : "+s"(kp), "+s"(vp) : "v"(voa), "s"(soa));
    }
    const rsrc_t ra = mk(kp, 8 * 65536 * 256);
    slp<KVD>();
    const rsrc_t rb = mk(vp, 8 * 65536 * 256);
#pragma unroll
    for (int n = 0; n < WK1 - VF; ++n) {
      R[n] = ld128<AUX_NT>(ra, voa + (n & 1) * 128, soa + (n >> 1) * 131072);
      if (VF && n == VFP) { cfence(); R[8] = ld128<AUX_V>(rb, vob, soa); cfence(); }
    }
    cfence();
    SB();
    auto rest = [&]() AI {
#pragma unroll
      for (int n = WK1 - VF; n < 16; ++n) {
        if (VF && n == 8) continue;
        if (STEP == 1 || !(n & 1)) { if (n < WSW) vmw<WK1 - STEP>(); else vmw<WK2 - STEP>(); }
        if (n < 8) R[n] = ld128<AUX_NT>(ra, voa + (n & 1) * 128, soa + (n >> 1) * 131072);
        else R[n] = ld128<AUX_V>(rb, vob + (n & 1) * 256, soa + ((n - 8) >> 1) * 131072);
      }
      cfence();
    };
    if (KV1) { rest(); SB(); }
    asm volatile("s_branch 1f\n"
                 LP "gt1:\n\ts_mov_b64 exec, -1\n\ts_load_dwordx2 s[4:5], s[0:1], 0x30\n\t"
                 "v_and_b32 v142, 15, v0\n\tv_mov_b32 v145, 0\n\tds_write_b32 v145, v145 offset:%0\n\t"
                 "s_and_b32 m0, s2, 7\n\ts_lshl_b32 s8, m0, 2\n\tv_lshl_add_u32 v142, v142, 7, s8\n\ts_movk_i32 s9, %1\n\t"
                 "s_waitcnt lgkmcnt(0)\n\ts_memrealtime s[6:7]\n\ts_waitcnt lgkmcnt(0)\n\t"
                 "v_mov_b32 v144, s6\n\ts_cmpk_lt_u32 s2, 120\n\ts_cbranch_scc1 " LP "gt2\n\t"
                 "global_store_dword v142, v144, s[4:5] offset:%1 sc1\n\ts_branch " LP "gt2\n\t"
                 ".subsection 1\n\t.p2align 12\n1:" :: "n"(L_TR), "n"(WS_T0), "n"(T_PV));
    PAD(16, SP(7, "reth"));
    FENCE();
    IDS(TOFF);
    const int i = j - 16, idx = xcd * 16 + i;
    STAMP(14);
    EPOCH();
    if (!KV1) { rest(); STAMP(14); }
    COMMON();
    {
      STAMP(1);
      const int I = 2 * xcd + (i >> 3), J = i & 7;
      v4u wD[4];
      const rsrc_t rwo = mk(ARG(A_WO), 4096 * 512 * 2);
      u32 aq = L_Q + (c >> 2) * 256 + (2 * g + (c & 1)) * 16, am = L_ML + (c >> 2) * 32 + w * 4, al = L_LS + (c >> 2) * 576 + w * 64 + g * 16 + (c & 3) * 4;
      u32 ao = L_O + w * 2048 + g * 512 + c * 32, MA0 = (c & 2) ? 0u : hm, MA1 = (c & 2) ? hm : 0u, vw = rowl * 1024 + (c & 7) * 16;
      u32 cm = L_ML + (w == 4 ? (l & 31) : w * 8 + (l & 7)) * 4;
      u32 co = w == 4 ? L_LS + ((l >> 3) & 3) * 576 + (l & 7) * 64 + ((l >> 2) & 3) * 16 : L_O + w * 512 + l * 8;
      u32 cs = w == 4 ? (STL ? ((l & 39) ? OOB : l * 32) : ((l & 7) ? OOB : l * 2)) : l * 8, c1 = co ^ 16, c2 = co ^ 32, c3 = co ^ 48;
      u32 sc = w == 4 ? (STL ? WS_ST + xcd * 1024 + i * 16 : WS_ST + idx * 128) : WS_H3 + idx * 2048 + w * 512, sw = (256 * I + 32 * w) * 1024 + J * 128;
      const u32 tm = tagm();
      asm volatile("" : "+v"(aq), "+v"(am), "+v"(al), "+v"(ao), "+v"(MA0), "+v"(MA1), "+v"(vw), "+v"(cm), "+v"(co), "+v"(cs), "+v"(c1), "+v"(c2), "+v"(c3), "+s"(sc), "+s"(sw));
      auto ldwo = [&]() AI {
        cfence();
#pragma unroll
        for (int t = 0; t < 4; ++t) wD[t] = ld128<AUX_WO>(rwo, vw + 4096 * t, sw);
        cfence();
      };
      if (WO_POS == 0) { until(T_WO); ldwo(); }
      PAD(12, "reth");
      asm volatile("s_barrier" ::: "memory");
      STAMP(11);
      v2u pa[8];
      {
        v4u q0 = *(const v4u*)(lds + aq), q1 = *(const v4u*)(lds + aq + 128);
        if (WO_POS == 4) ldwo();
        asm volatile("" : "+v"(q0), "+v"(q1));
        v4f d[4];
#pragma unroll
        for (int q = 0; q < 4; ++q) d[q] = mfma32(R[2 * q], q0, Z4);
#pragma unroll
        for (int q = 0; q < 4; ++q) d[q] = mfma32(R[2 * q + 1], q1, d[q]);
        if (WO_POS == 5) { SB(); ldwo(); SB(); }
        float ka[4], kb[4];
#pragma unroll
        for (int q = 0; q < 4; ++q) {
          if (q & 1) { ka[q] = d[q].y + dppf<QP(0, 0, 2, 2)>(d[q].x); kb[q] = d[q].w + dppf<QP(0, 0, 2, 2)>(d[q].z); }
          else { ka[q] = d[q].x + dppf<QP(1, 1, 3, 3)>(d[q].y); kb[q] = d[q].z + dppf<QP(1, 1, 3, 3)>(d[q].w); }
        }
        const float ra = (c & 2) ? ((c & 1) ? ka[3] : ka[2]) : ((c & 1) ? ka[1] : ka[0]);
        const float rb_ = (c & 2) ? ((c & 1) ? kb[3] : kb[2]) : ((c & 1) ? kb[1] : kb[0]);
        const float s0 = rbf(rbf(ra) * 0.08838834764831845f), s1 = rbf(rbf(rb_) * 0.08838834764831845f);
        float mw = fmx(s0, s1);
        MAXD(mw, Q1032); MAXD(mw, Q2301);
        mw = sw32max(sw16max(mw)) * 1.4426950408889634f;
        const float e0 = __builtin_amdgcn_exp2f(fmaf(s0, 1.4426950408889634f, -mw)), e1 = __builtin_amdgcn_exp2f(fmaf(s1, 1.4426950408889634f, -mw));
        const u32 d0 = dup(e0), d1 = dup(e1);
        pa[0] = v2u{dppu<QP(0, 0, 0, 0)>(d0) & MA0, dppu<QP(0, 0, 0, 0)>(d0) & MA1}; pa[1] = v2u{dppu<QP(0, 0, 0, 0)>(d1) & MA0, dppu<QP(0, 0, 0, 0)>(d1) & MA1};
        pa[2] = v2u{dppu<QP(1, 1, 1, 1)>(d0) & MA0, dppu<QP(1, 1, 1, 1)>(d0) & MA1}; pa[3] = v2u{dppu<QP(1, 1, 1, 1)>(d1) & MA0, dppu<QP(1, 1, 1, 1)>(d1) & MA1};
        pa[4] = v2u{dppu<QP(2, 2, 2, 2)>(d0) & MA0, dppu<QP(2, 2, 2, 2)>(d0) & MA1}; pa[5] = v2u{dppu<QP(2, 2, 2, 2)>(d1) & MA0, dppu<QP(2, 2, 2, 2)>(d1) & MA1};
        pa[6] = v2u{dppu<QP(3, 3, 3, 3)>(d0) & MA0, dppu<QP(3, 3, 3, 3)>(d0) & MA1}; pa[7] = v2u{dppu<QP(3, 3, 3, 3)>(d1) & MA0, dppu<QP(3, 3, 3, 3)>(d1) & MA1};
        *(float*)(lds + am) = mw; *(float*)(lds + al) = e0 + e1;
      }
      STAMP(4);
      if (WO_POS == 1) ldwo();
      {
        v4f a0 = Z4, a1 = Z4;
#pragma unroll
        for (int k = 0; k < 8; ++k) {
          const v4u v = R[8 + k];
          a0 = mfma16(pa[k], v2u{v.x, v.y}, a0);
          a1 = mfma16(pa[k], v2u{v.z, v.w}, a1);
        }
        v4f* p = (v4f*)(lds + ao);
        p[0] = a0; p[1] = a1;
      }
      STAMP(5);
      if (WO_POS == 2) ldwo();
      STAMP(6);
      PAD(11, "reth");
      bar();
      if (w < 5) {
        if (w == 4) {
          cfence();
          const float m = *(const float*)(lds + cm);
          const v4f p0 = *(const v4f*)(lds + co), p1 = *(const v4f*)(lds + c1), p2 = *(const v4f*)(lds + c2), p3 = *(const v4f*)(lds + c3);
          SB();
          float M = m; MAXD(M, Q1032); MAXD(M, Q2301); MAXD(M, "row_half_mirror");
          const v4f p = (p0 + p1) + (p2 + p3);
          float L = ((p.x + p.z) + (p.y + p.w)) * __builtin_amdgcn_exp2f(m - M);
          L += dppf<QP(1, 0, 3, 2)>(L); L += dppf<QP(2, 3, 0, 1)>(L); L += dppf<HMIR>(L);
#pragma unroll
          for (int q = 0; q <= (CSX ? N3 : 0); ++q) st128(ws, v4u{asu(M), T, asu(L), T}, cs, sc + q * WS_CS);
          STAMP(8);
        } else {
          STAMP(3);
          const float m = *(const float*)(lds + cm);
          v2f x[8];
#pragma unroll
          for (int k = 0; k < 8; ++k) x[k] = *(const v2f*)(lds + co + k * 2048);
          SB();
          float M = m; MAXD(M, Q1032); MAXD(M, Q2301); MAXD(M, "row_half_mirror");
          const float e = __builtin_amdgcn_exp2f(m - M);
          v2f s0 = x[0] * dppf<0x150>(e), s1 = x[1] * dppf<0x151>(e);
          s0 += x[2] * dppf<0x152>(e); s1 += x[3] * dppf<0x153>(e);
          s0 += x[4] * dppf<0x154>(e); s1 += x[5] * dppf<0x155>(e);
          s0 += x[6] * dppf<0x156>(e); s1 += x[7] * dppf<0x157>(e);
          s0 += s1;
#pragma unroll
          for (int q = 0; q <= (CSX ? N3 : 0); ++q) st64(ws, v2u{enc(s0.x, TP, tm), enc(s0.y, TP, tm)}, cs, sc + q * WS_CS);
          STAMP(8);
        }
      }
      cfence();
      const u32 tb = now() - (w < 5 ? DH3C : 0);
      STAMP(7);
      if (WO_POS == 3) ldwo();
      PAD(13, SP(8, "reth"));
      const int hl = 2 * (i >> 3) + (w >> 2);
      u32 ar = L_RD + g * 1024 + c * 16, vr = c * 16 + g * 4, sr = WS_H4 + I * 2048 + J * 256;
      v4u x3[N3 + 1][4];
      {
        v4u y[2], z[2];
        constexpr u32 STS = STL ? 128 : 1024;
        u32 vs = (l >> 3) * (STL ? 16 : 128), sos = STL ? WS_ST + xcd * 1024 + hl * 256 : WS_ST + xcd * 2048 + hl * 16;
        u32 vh = (l >> 3) * 2048 + (l & 7) * 16, vh2 = vh + 16384, soh = WS_H3 + xcd * 32768 + hl * 512 + (w & 3) * 128;
        u32 ad = L_RD + (w * 128 + col2) * 4;
        v4u MD = MK;
        asm volatile("" : "+v"(vs), "+v"(vh), "+v"(vh2), "+v"(ad), "+v"(MD), "+v"(ar), "+v"(vr), "+s"(sos), "+s"(soh), "+s"(sr));
        if (GT && T_H3) t_start = __builtin_amdgcn_readfirstlane(*(const u32*)(lds + L_TK));
        until(T_H3);
        while (now() - tb < D_H3) __builtin_amdgcn_s_sleep(1);
        cfence();
        STAMP(12);
        y[0] = ld128<AUX_PL>(ws, vs, sos); y[1] = ld128<AUX_PL>(ws, vs + STS, sos);
        z[0] = ld128<AUX_PL>(ws, vh, soh); z[1] = ld128<AUX_PL>(ws, vh2, soh);
        if (N3) {
          const u32 t1 = now();
#pragma unroll
          for (int q = 0; q < N3; ++q) {
            while (now() - t1 < (u32)(D3 * (q + 1))) __builtin_amdgcn_s_sleep(1);
            cfence();
            x3[q][0] = ld128<AUX_PL>(ws, vs, sos + (q + 1) * CSX); x3[q][1] = ld128<AUX_PL>(ws, vs + STS, sos + (q + 1) * CSX);
            x3[q][2] = ld128<AUX_PL>(ws, vh, soh + (q + 1) * CSX); x3[q][3] = ld128<AUX_PL>(ws, vh2, soh + (q + 1) * CSX);
            cfence();
          }
        }
        float e0, e1;
        auto fac = [&]() AI {
          float M = fmx(asf(y[0].x), asf(y[1].x));
          MAXD(M, "row_ror:8"); M = sw32max(sw16max(M));
          e0 = __builtin_amdgcn_exp2f(asf(y[0].x) - M); e1 = __builtin_amdgcn_exp2f(asf(y[1].x) - M);
          float L = fmaf(e0, asf(y[0].z), e1 * asf(y[1].z));
          L += dppf<ROR8>(L); L = sw16add(L, L); L = __builtin_amdgcn_rcpf(sw32add(L, L));
          e0 *= L; e1 *= L;
        };
        auto mix = [&]() AI {
          const v4f s = asf4(z[0]) * e0 + asf4(z[1]) * e1;
          const float v = tred4(s.x, s.y, s.z, s.w);
          return dup(v + dppf<ROR8>(v));
        };
        fac();
        u32 any = diff<true>(y[0], T) | diff<true>(y[1], T);
        asm volatile("" : "+v"(e0), "+v"(e1), "+v"(any));
        SB();
        z[0] = untag(z[0], TP); z[1] = untag(z[1], TP);
        asm volatile("" : "+v"(z[0]), "+v"(z[1]));
        any |= (or4(z[0]) | or4(z[1])) & TAGM;
        u32 o = mix();
        asm volatile("" : "+v"(o));
        if (__builtin_expect(__builtin_amdgcn_ballot_w64(any != 0) != 0, 0)) {
          SPAD(8);
          STAMP(13);
          u32 spin = 0;
          bool more = true;
#pragma unroll
          for (int q = 0; q < N3; ++q) {
            if (more) {
              cfence();
              const bool b0 = diff<true>(y[0], T) != 0, b1 = diff<true>(y[1], T) != 0, b2 = (or4(z[0]) & TAGM) != 0, b3 = (or4(z[1]) & TAGM) != 0;
              y[0] = b0 ? x3[q][0] : y[0]; y[1] = b1 ? x3[q][1] : y[1]; z[0] = b2 ? untag(x3[q][2], TP) : z[0]; z[1] = b3 ? untag(x3[q][3], TP) : z[1];
              any = (diff<true>(y[0], T) | diff<true>(y[1], T)) | ((or4(z[0]) | or4(z[1])) & TAGM);
              more = __builtin_amdgcn_ballot_w64(any != 0) != 0;
            }
          }
          if (more)
#pragma unroll 1
          do {
            if (S3) until(T_H3 + (spin < 64 ? (int)spin + 1 : 0) * S3);
            cfence();
            const bool b0 = diff<true>(y[0], T) != 0, b1 = diff<true>(y[1], T) != 0, b2 = (or4(z[0]) & TAGM) != 0, b3 = (or4(z[1]) & TAGM) != 0;
            const v4u n0 = ld128<AUX_PL>(ws, b0 ? vs : OOB, sos), n1 = ld128<AUX_PL>(ws, b1 ? vs + STS : OOB, sos);
            const v4u n2 = ld128<AUX_PL>(ws, b2 ? vh : OOB, soh), n3 = ld128<AUX_PL>(ws, b3 ? vh2 : OOB, soh);
            y[0] = b0 ? n0 : y[0]; y[1] = b1 ? n1 : y[1]; z[0] = b2 ? untag(n2, TP) : z[0]; z[1] = b3 ? untag(n3, TP) : z[1];
            any = (diff<true>(y[0], T) | diff<true>(y[1], T)) | ((or4(z[0]) | or4(z[1])) & TAGM);
          } while (__builtin_amdgcn_ballot_w64(any != 0) != 0 && ++spin < SPIN_MAX);
          fac();
          o = mix();
        }
        STAMP(9);
        half2(gemv4(MD, o, wD), (float*)(lds + ad));
      }
      bar();
      if (w == 0) {
        const v4f* p = (const v4f*)(lds + ar);
        const v4f s = p[0] + p[32];
        const u32 r4 = enc(tred4(s.x, s.y, s.z, s.w), TP, tm);
#pragma unroll
        for (int q = 0; q <= (CSX ? N4 : 0); ++q) st32(ws, r4, vr, sr + q * WS_CS);
      }
      STAMP(10);
      cfence();
      keep<4, N3>(x3);
      POUT();
      ENDL(1);
    }
  }
}

ROLE(role_qa) {
  PROLOG();
  {
    v4u xv, cv_;
    u32 ep_, vob, sob, p1 = 0;
    rsrc_t rb;
    {
      typedef const __attribute__((address_space(1))) char* gp;
      const u32 t0 = __builtin_amdgcn_readfirstlane(tid);
      const u32 ra0 = ((blk >> 4) << 6) + (t0 >> 3);
      const u32 voa = (tid & 7) * 16 + ((tid >> 4) & 3) * 6144 + (tid & 8) * 3072, soa = ra0 * 6144 + ((blk & 15) << 7);
      char *mp = ARG(A_MOD), *wp = ARG(A_WS), *cp = ARG(A_C), *qp = ARG(A_WQ);
      gp xp = (gp)ka[A_X];
      asm volatile("" : "+s"(mp), "+s"(wp), "+s"(cp), "+s"(qp), "+s"(xp) : "v"(voa), "s"(soa));
      R[0] = ld128<AUX_NT>(mk(mp, 512 * 3072 * 2), voa, soa);
      asm volatile("s_load_dwordx4 %0, %2, %3\n\ts_load_dword %1, %4, 0x0" : "=&s"(cv_), "=&s"(ep_) : "s"(cp), "s"(2 * ra0), "s"(wp));
      asm volatile("s_memrealtime s[92:93]" :: "s"(cp));
      cfence();
      xv = *(const __attribute__((address_space(1))) v4u*)(xp + (tid & 63) * 16);
      cfence();
      rb = mk(qp, 512 * 4096 * 2);
      vob = ((tid & 48) << 9) + (tid & 15) * 16; sob = ((blk >> 5) << 20) + (t0 << 11) + ((blk & 7) << 10) + ((blk & 24) << 5);
#pragma unroll
      for (int t = 0; t < QW0; ++t) R[4 + t] = ld128<AUX_NT>(rb, vob, sob + t * 32768);
      if (PT1) {
        cfence();
        p1 = ld32<AUX_PL>(mk(wp, WS_H2), (tid & ~7) == PT1 * 64 ? (tid & 7) * 64 : OOB, WS_H1 + blk * 512);
      }
    }
    cfence();
    FENCE();
    IDS(TOFQ);
    const int kb = blk >> 5, head = 4 * xcd + (j & 3), r0 = 128 * kb + 16 * w;
    const int ks = kb, cbk = blk & 31;
    const int e = 8 * xcd + j - 8 - (xcd > 4 ? 2 * (xcd - 4) : 0);
    const bool ffn = j >= 8 && (xcd < 4 || j < 14);
    COMMON();
    {
      PAD(20, "reti");
      u32 cv[4];
#pragma unroll
      for (int k = 0; k < 4; ++k) asm volatile("v_mov_b32 %0, %1" : "=v"(cv[k]) : "s"(cv_[k]));
      const u32 c0 = a ? (g >= 2 ? cv[3] : cv[2]) : (g >= 2 ? cv[1] : cv[0]);
      v4u A0 = MK & dup(silu_bf((g & 1) ? c0 >> 16 : c0));
      asm volatile("" : "+v"(A0));
      STAMP(1);
      half2(mfma32(A0, R[0], Z4), ldsf + L_RA / 4 + w * 128 + col2);
    }
    STAMP(2);
    if (QWP) {
      cfence();
#pragma unroll
      for (int t = QW0; t < 4; ++t) R[4 + t] = ld128<AUX_NT>(rb, vob, sob + t * 32768);
      cfence();
    }
    bar();
    if (w == 0) {
      const v4f* p = (const v4f*)(lds + L_RA) + g * 64 + c;
      const v4f s = p[0] + p[32];
      st64(ws, v2u{asu(tred4(s.x, s.y, s.z, s.w)), T}, (c >> 2) * 256 + (g >> 1) * 128 + (c & 3) * 16 + (g & 1) * 8, WS_H1 + (blk >> 4) * 8192 + (blk & 7) * 1024 + (blk & 8) * 8);
      STAMP(3);
    }
    STAMP(11);
    cfence();
#pragma unroll
    for (int t = QWP ? 4 : QW0; t < 4; ++t) R[4 + t] = ld128<AUX_NT>(rb, vob, sob + t * 32768);
    if ((PW & 4) && w < 2) st64(ws, v2u{~T, ~T}, c * 32 + g * 8, WS_H2 + kb * 32768 + head * 1024 + w * 512);
    const u32 xr = ld16(rx, (4 * (c & 3) + g) * 2, r0 * 2);
    u32 aM = ld16(rc, rowa * 2, (256 * (ks & 1) + 32 * w) * 2);
    cfence();
    STAMP(8);
    STAMP(9);
    const rsrc_t re = ffn ? mk(ARG(A_WD), 1792 * 512 * 2) : rmod;
    const u32 pit = ffn ? 1024 : 6144;
    const u32 soe = ffn ? 32 * e * 1024 + w * 128 : (256 * ks + 32 * w) * 6144 + 2048 + cbk * 128;
    auto lde = [&](bool on) AI {
      cfence();
#pragma unroll
      for (int t = 0; t < 4; ++t) R[8 + t] = ld128<AUX_NT>(re, on ? rowl * pit + (c & 7) * 16 : OOB, soe + 4 * t * pit);
      cfence();
    };
    {
      float mean, ri;
      {
        const u32 ONE = 0x3f803f80u;
        float s = dot2(xv.x, ONE, dot2(xv.y, ONE, 0.f)) + dot2(xv.z, ONE, dot2(xv.w, ONE, 0.f));
        mean = wavesum(s) * (1.f / 512.f);
        PADV(21, "reti", mean);
        float q = 0.f;
        const u32 xs[4] = {xv.x, xv.y, xv.z, xv.w};
#pragma unroll
        for (int k = 0; k < 4; ++k) {
          const float d0 = asf(xs[k] << 16) - mean, d1_ = asf(xs[k] & 0xffff0000u) - mean;
          q = fmaf(d0, d0, fmaf(d1_, d1_, q));
        }
        const float var = rbf(wavesum(q) * (1.f / 512.f));
        ri = __builtin_amdgcn_rcpf(rbf(__builtin_amdgcn_sqrtf(rbf(var + 1e-5f))));
        mean = rbf(mean);
      }
      asm volatile("" : "+v"(mean), "+v"(ri));
      STAMP(12);
      if (GT) {
        u32 tr, t;
        for (;;) {
          cfence();
          tr = __builtin_amdgcn_readfirstlane(*(const u32*)(lds + L_TR)); t = now();
          if ((tr && t - tr >= T_H1) || t - t_start >= T_H1F) break;
          __builtin_amdgcn_s_sleep(1);
        }
      } else until(T_H1);
      v4u y[2];
      const u32 vo[2] = {(u32)((l >> 3) * 8192 + (l & 7) * 16), (u32)((l >> 3) * 8192 + (l & 7) * 16 + 128)};
      cfence();
      y[0] = ld128<AUX_PL>(ws, vo[0], WS_H1 + r0 * 16);
      y[1] = ld128<AUX_PL>(ws, vo[1], WS_H1 + r0 * 16);
      if (MR_EARLY || DN_EARLY) lde((MR_EARLY && j < 8) || (DN_EARLY && ffn));
      float xn = rbf(rbf(asf(xr << 16) - mean) * ri);
      asm volatile("" : "+v"(xn) : "v"(p1));
      STAMP(13);
      settle<2, true, 1>(y, ws, vo, WS_H1 + r0 * 16, T);
      STAMP(15);
      float v = tred4(asf(y[0].x), asf(y[0].z), asf(y[1].x), asf(y[1].z));
      v = rbf(v + dppf<ROR8>(v));
      const u32 hd = dup(rbf(xn * rbf(1.f + dppf<ROR12>(v))) + v);
      const v4u wa[4] = {R[4], R[5], R[6], R[7]};
      const v4f acc = gemv4(MK, hd, wa);
      *(v4f*)(ldsf + L_RB / 4 + w * 256 + (g & 1) * 128 + 8 * c + 4 * (g >> 1)) = acc;
    }
    STAMP(4);
    bar();
    if (w < 2) {
      const v4f* p = (const v4f*)(lds + L_RB) + g * 128 + w * 16 + c;
      const v4f s = p[0] + p[64];
      st64(ws, v2u{asu(tred4(s.x, s.y, s.z, s.w)), T}, c * 32 + g * 8, WS_H2 + kb * 32768 + head * 1024 + w * 512);
    }
    STAMP(5);
    PAD(22, "reti");
    if (GT) {
      cfence();
      const u32 tr = __builtin_amdgcn_readfirstlane(*(const u32*)(lds + L_TR));
      t_start = tr ? tr : t_start + 30;
    }
    u32 pf, pf2 = 0;
    {
      const bool pd = (DNPF == 1 || DNPF == 2) && w < 4, pw = WOPF == 1 ? w < 4 : WOPF == 3 && j < 8;
      const bool ut = UT && ffn && w < 2;
      const rsrc_t rp = mk(pw ? ARG(A_WO) : pd ? ARG(A_WD) : (GUP ? w < 4 : ut ? w == 0 : j < 8) ? ARG(A_WG) : ARG(A_WU), pw ? 4096 * 512 * 2 : 512 * 1792 * 2);
      const u32 t = tid & 255;
      u32 vp = (!GU_PF || (GUP ? j >= 8 : w < 4) || (xcd >= 4 && (t & 3) == 3)) ? OOB : (t >> 2) * 3584 + (t & 3) * 128;
      u32 sp = (j & 7) * 64 * 3584 + (4 * xcd - (xcd > 4 ? xcd - 4 : 0)) * 128;
      if (pd) { vp = (j < (xcd < 4 ? 8 : 6)) ? t * 128 : OOB; sp = (e + 8) * 32768; }
      if (pw) { vp = t * 128; sp = (16 * xcd + (WOPF == 3 ? 2 * j + (w >> 2) : j)) * 32768; }
      if (ut) { vp = l * 28672; sp = e * 64; }
      if (MR_FIRST && j < 8) { until(T_MR); lde(true); }
      if (!((GUP || WOPF == 3) && j >= 8)) until((pw ? T_PW : pd ? T_DN : T_PG) + (j >= 8 ? (pw ? PDL : PDG) : 0));
      cfence();
      pf = ld32<AUX_PF>(rp, vp, sp);
      cfence();
      if (WOPF == 4 && j < 8) {
        until(T_PW);
        pf2 = ld32<AUX_PF>(mk(ARG(A_WO), 4096 * 512 * 2), t * 128, (16 * xcd + 2 * j + (w >> 2)) * 32768);
        cfence();
      }
    }
    if (j < 8) {
      PAD(5, RW);
      if (!MR_EARLY && !MR_FIRST) { until(T_MR); lde(true); }
      {
        const v4u wM[4] = {R[8], R[9], R[10], R[11]};
        aM = dup(silu_bf(aM));
        asm volatile("" : "+v"(aM));
        PADV(8, SP(4, RW), aM);
        half2(gemv4(MK, aM, wM), ldsf + L_RM / 4 + w * 128 + col2);
        STAMP(6);
        bar();
        if (w == 0) {
          const v4f* p = (const v4f*)(lds + L_RM) + g * 64 + c;
          const v4f s = p[0] + p[32];
          st32(ws, enc(tred4(s.x, s.y, s.z, s.w), TP, tagm()), c * 16 + g * 4, WS_MR + ks * 8192 + cbk * 256);
        }
        STAMP(7);
      }
      if (WOPF == 5) {
        until(T_PW);
        cfence();
        pf2 = ld32<AUX_PF>(mk(ARG(A_WO), 4096 * 512 * 2), (tid & 255) * 128, (16 * xcd + 2 * j + (w >> 2)) * 32768);
        cfence();
      }
      if (DNPF >= 4 && w >= 4) {
        until(T_DN);
        cfence();
        pf |= ld32<AUX_PF>(mk(ARG(A_WD), 1792 * 512 * 2), (j < (xcd < 4 ? 8 : 6)) ? (tid & 255) * 128 : OOB, (e + 8) * 32768);
        cfence();
      }
      STAMP(14);
      if (blk < 32 && w == 0) {
        const int r = blk;
        const int col = 4 * (c & 3) + g;
        const u32 x2 = ld16(rx, col * 2, r * 32);
        const rsrc_t ro = mk(ARG(A_OUT), 1024);
        u32 ot = 0;
        if (OTB) ot = ld16(ro, col * 2, r * 32);
        float ga, gb, x1;
        {
          until(T_MG);
          v4u y[1];
          const u32 vo[1] = {(u32)((g & 1) * 8192 + (c >> 3) * 6144 + (c & 3) * 16)};
          gather<1, false, 4>(y, ws, vo, WS_MR + r * 64, TP);
          auto q = __builtin_amdgcn_permlane32_swap(asu(sw16add(asf(y[0].x), asf(y[0].y))), asu(sw16add(asf(y[0].z), asf(y[0].w))), false, false);
          ga = rbf(asf(q[0]));
          gb = dppf<ROR8>(ga);
        }
        STAMP(11);
        PAD(6, SP(5, RW));
        const u32 v0 = (l >> 2) * 2048 + (l & 3) * 16;
        {
          float xf = asf(x2 << 16);
          asm volatile("" : "+v"(xf), "+v"(ga), "+v"(gb) : "v"(ot));
          until(T_H4O);
          const u32 vo[1] = {v0};
          v4u x0[1][1];
          x1 = asf(gath<1, 5, 0>(ws, vo, WS_H4 + r * 64, TP, TP, [&](v4f y) AI {
            float s = tred4(y.x, y.y, y.z, y.w);
            s += dppf<ROR8>(s); s += dppf<ROR4>(s);
            return asu(rbf(xf + rbf(ga * rbf(s))));
          }, [] {}, [](int) {}, x0 GA));
          STAMP(8);
          if (r == 0) st32(ws, T, l ? OOB : 0, WS_EP);
        }
        const u32 th4 = now(); (void)th4;
        PAD(7, SP(6, RW));
        {
          const bool in = l < 32;
          u32 vo[4] = {v0, v0 + 32768, v0 + 65536, in ? v0 + 98304 : OOB}, tl = in ? TP : 0u, vq = c < 4 ? col * 2 : OOB, s5 = WS_H5 + r * 64, sq = r * 32;
          asm volatile("" : "+v"(vo[0]), "+v"(vo[1]), "+v"(vo[2]), "+v"(vo[3]), "+v"(tl), "+v"(vq), "+s"(s5), "+s"(sq) : "s"(ro));
          until(T_H5);
          if (D_H5) { while (now() - th4 < D_H5) __builtin_amdgcn_s_sleep(1); }
          auto fo = [&](v4f y) AI {
            float s = tred4(y.x, y.z, y.y, y.w);
            s += dppf<ROR8>(s); s += dppf<ROR4>(s);
            return dup(x1 + rbf(gb * rbf(s)));
          };
          if (RING) ring<4, 6>(ws, vo, s5, TP, tl, fo, [&](u32 o) AI { st16(ro, o, vq, sq); STAMP(10); } GA);
          else {
            v4u x5[N5 + 1][4];
            const u32 o = gath<4, 6, N5>(ws, vo, s5, TP, tl, fo, [] {}, [&](int q) AI { until(T_H5 + q * D5); }, x5 GA);
            STAMP(9);
            st16(ro, o, vq, sq);
            cfence();
            STAMP(10);
          }
        }
      }
      asm volatile("" :: "v"(pf), "v"(pf2));
      POUT();
      ENDL(0);
    }
    if (!ffn) { asm volatile("" :: "v"(pf)); POUT(); END(); }
    {
      const rsrc_t rg = mk((e & 1) ? ARG(A_WU) : ARG(A_WG), 512 * 1792 * 2), ru = mk((e & 1) ? ARG(A_WG) : ARG(A_WU), 512 * 1792 * 2);
      const u32 x2 = ld16(rx, (4 * c + g) * 2, w * 128);
      u32 vg = (l >> 2) * 3584 + (l & 3) * 16, sg[4], sn[4];
      const u32 aw = L_W + w * 8192 + (e & 1) * 4096;
#pragma unroll
      for (int n = 0; n < 4; ++n) { sg[n] = (64 * w + 16 * n) * 3584 + e * 64; sn[n] = 32 * e * 1024 + w * 256 + n * 4096; asm volatile("" : "+s"(sg[n]), "+s"(sn[n])); }
      u32 at = L_W + w * 8192 + (4 * (c >> 2) + g) * 64 + (c & 3) * 8;
      u32 vom[2] = {(u32)((g & 1) * 8192 + (g >> 1) * 2048 + c * 16 + 2048), (u32)((g & 1) * 8192 + c * 16)}, sm = WS_MR + w * 256;
      u32 vn = rowl * 1024 + (c & 7) * 16;
      u32 gsel = ((c >> 3) * 32 + rowa) * 16, psel = (c & 1) ? 0x03020706u : 0x05040100u;
      u32 vd = c * 32 + (g >> 1) * 16 + (g & 1) * 8, sd = WS_H5 + e * 2048 + w * 512, ax = L_SX + c * 4;
      const u32 v0 = g * 2048 + c * 16;
      u32 vo[4] = {v0, v0 + 8192, v0 + 16384, v0 + 24576}, s4 = WS_H4 + w * 256, as = L_SX + ((g & 1) ? (l >> 5) * 32 : 64) + w * 4;
      u32 a12 = L_G1 + (w >> 1) * 1024 + c * 16 + (w & 1) * 8, a3 = L_G3 + (w >> 2) * 1024 + c * 16 + (w & 3) * 4;
      v4u MD = MK;
      const u32 tm = tagm();
      constexpr u32 SENT = 0xffffffffu;
      *(u32*)(lds + as) = SENT;
      asm volatile("" : "+v"(vg), "+v"(at), "+v"(vom[0]), "+v"(vom[1]), "+v"(vn), "+v"(gsel), "+v"(psel), "+v"(vd), "+v"(ax), "+s"(sm), "+s"(sd));
      asm volatile("" : "+v"(vo[0]), "+v"(vo[1]), "+v"(vo[2]), "+v"(vo[3]), "+v"(as), "+v"(a12), "+v"(a3), "+v"(MD), "+s"(s4) : "s"(rg), "s"(ru), "s"(re));
      if ((PW & 2) && w < 4) {
#pragma unroll
        for (int q = 0; q <= C5; ++q) st64(ws, v2u{~T, ~T}, vd, sd + q * WS_CS);
      }
      v4u wd[8];
      auto lddn = [&]() AI {
        cfence();
        if (w < 4) {
#pragma unroll
          for (int t = 0; t < 8; ++t) wd[t] = ld128<AUX_DN>(re, vn + (t >> 2) * 128, sn[t & 3]);
        }
        cfence();
      };
      if (!DNPF) { until(T_DN); lddn(); }
      if (PFW) asm volatile("" :: "v"(pf));
      STAMP(13);
      until(T_GU);
      cfence();
#pragma unroll
      for (int n = 0; n < 8; ++n) {
        if (HALF == 1 && n == 4) vmw<0>();
        if (HALF > 1 && n == 4) until(HALF);
        __builtin_amdgcn_raw_ptr_buffer_load_lds(n < 4 ? rg : ru, (__attribute__((address_space(3))) void*)(size_t)((aw ^ ((n >> 2) * 4096)) + (n & 3) * 1024), 16, vg, sg[n & 3], 0, AUX_GU);
      }
      cfence();
      if (DNPF == 1 || DNPF == 5) lddn();
      STAMP(14);
      PAGE(2);
      PAD(1, SP(2, RF("pad2")));
      v4u ym[2];
      until(T_MG);
      cfence();
      ym[0] = ld128<AUX_PL>(ws, vom[0], sm); ym[1] = ld128<AUX_PL>(ws, vom[1], sm);
      cfence();
      vmw<2>();
      v4u bw[8];
#pragma unroll
      for (int t = 0; t < 8; ++t) {
        typedef __attribute__((address_space(3))) v4s* lp;
        const u32 b = at + (t >> 2) * 4096 + (t & 1) * 2048 + ((t >> 1) & 1) * 32;
        const v2u lo = __builtin_bit_cast(v2u, __builtin_amdgcn_ds_read_tr16_b64_v4i16((lp)(size_t)b));
        const v2u hi = __builtin_bit_cast(v2u, __builtin_amdgcn_ds_read_tr16_b64_v4i16((lp)(size_t)(b + 1024)));
        bw[t] = v4u{lo.x, lo.y, hi.x, hi.y};
      }
      if (!PFW) asm volatile("" :: "v"(pf));
      STAMP(6);
      PAD(2, SP(3, RF("pad3")));
      auto rows = [&](u32 p, v4u& A0, v4u& A1) AI {
        const u32 v = __builtin_amdgcn_perm(dppu<QP(1, 0, 3, 2)>(p), p, psel);
        A0 = v4u{v, dppu<0x12E>(v), dppu<0x12C>(v), dppu<0x12A>(v)};
        A1 = v4u{dppu<0x128>(v), dppu<0x126>(v), dppu<0x124>(v), dppu<0x122>(v)};
      };
      float cb1, ga, xf;
      v2f S12 = {0.f, 0.f};
      v4u B0, B1;
      {
        v4u (&y)[2] = ym;
        settle<2, false, 2>(ym, ws, vom, sm, TP);
        STAMP(11);
        ga = rbf(asf(__builtin_amdgcn_permlane32_swap(asu(sw16add(asf(y[1].x), asf(y[1].y))), asu(sw16add(asf(y[1].z), asf(y[1].w))), false, false)[0]));
        auto r = __builtin_amdgcn_permlane32_swap(asu(sw16add(asf(y[0].x), asf(y[0].y))), asu(sw16add(asf(y[0].z), asf(y[0].w))), false, false);
        const u32 cs = cvtpk(1.f + rbf(asf(r[1])), asf(r[0]));
        cb1 = asf(cs << 16);
        xf = asf(x2 << 16);
        rows(cs, B0, B1);
        asm volatile("" : "+v"(B0), "+v"(B1), "+v"(ga), "+v"(cb1), "+v"(xf));
        if (DNPF == 2 || DNPF == 3 || DNPF == 4) lddn();
      }
      auto spass = [&]() AI {
        SB();
        v4f d[4];
#pragma unroll
        for (int k = 0; k < 4; ++k) d[k] = mfma32(B0, bw[2 * k], Z4);
#pragma unroll
        for (int k = 0; k < 4; ++k) d[k] = mfma32(B1, bw[2 * k + 1], d[k]);
        if (l < 16) {
#pragma unroll
          for (int k = 0; k < 4; ++k) *(v2f*)(lds + a12 + k * 256) = v2f{d[k].x, d[k].y};
        }
        asm volatile("" :: "v"(d[0]), "v"(d[1]), "v"(d[2]), "v"(d[3]));
        bar();
        if (w < 4) {
          const v4f* p = (const v4f*)(lds + L_G1 + gsel);
          const v4f s = (p[0] + p[64]) + (p[128] + p[192]);
          S12 = v2f{s.x, s.y} + v2f{s.z, s.w};
        }
        asm volatile("" : "+v"(S12));
        SB();
      };
      if (SPRE) spass();
      STAMP(7);
      PAD(3, RF("pad4"));
      float mean = 0.f, ri = 0.f;
      auto stats = [&]() AI {
        cfence();
        float st = *(const float*)(lds + ax);
        const bool ok = __builtin_amdgcn_ballot_w64(asu(st) == SENT) == 0;
        st += dppf<QP(1, 0, 3, 2)>(st); st += dppf<QP(2, 3, 0, 1)>(st); st += dppf<HMIR>(st);
        const float m = asf(__builtin_amdgcn_readlane(asu(st), 0)) * (1.f / 512.f);
        const float msq = asf(__builtin_amdgcn_readlane(asu(st), 8)) * (1.f / 512.f);
        const float var = rbf(fmx(msq - m * m, 0.f));
        ri = __builtin_amdgcn_rcpf(rbf(__builtin_amdgcn_sqrtf(rbf(var + 1e-5f))));
        mean = rbf(m);
        asm volatile("" : "+v"(mean), "+v"(ri));
        return ok;
      };
      bool sok = SSP == 0;
      v4u x4[N4 + 1][4];
      {
        until(T_H4);
        const float x1 = asf(gath<4, 3, N4>(ws, vo, s4, TP, TP, [&](v4f y) AI { return asu(rbf(xf + rbf(ga * rbf(tred4(y.x, y.y, y.z, y.w))))); },
                                            [&]() AI { if (!SPRE) spass(); }, [&](int q) AI { until(T_H4 + q * D4); }, x4 GA));
        STAMP(8);
        const float u = x1 * cb1;
        v4u A0, A1;
        rows(cvtpk(u, u - rbf(u)), A0, A1);
        v4f d[4];
        float q = sw32add(x1, x1 * x1);
#define ADDD(ctl) do { SB(); asm("s_nop 0\n\tv_add_f32_dpp %0, %0, %0 " ctl " bank_mask:0xf" : "+v"(q)); SB(); } while (0)
        SB();
        d[0] = mfma32(A0, bw[0], Z4);
        d[1] = mfma32(A0, bw[2], Z4);
        ADDD("row_ror:8 row_mask:0xf");
        d[2] = mfma32(A0, bw[4], Z4);
        ADDD("row_ror:4 row_mask:0xf");
        d[3] = mfma32(A0, bw[6], Z4);
        ADDD(Q2301 " row_mask:0xf");
        d[0] = mfma32(A1, bw[1], d[0]);
        ADDD(Q1032 " row_mask:0xf");
        d[1] = mfma32(A1, bw[3], d[1]);
        ADDD("row_bcast:15 row_mask:0xa");
        d[2] = mfma32(A1, bw[5], d[2]);
        *(float*)(lds + as) = q;
        d[3] = mfma32(A1, bw[7], d[3]);
        SB();
        if (l < 16) {
#pragma unroll
          for (int k = 0; k < 4; ++k) *(float*)(lds + a3 + k * 256) = d[k].x + d[k].y;
        }
        asm volatile("" :: "v"(d[0]), "v"(d[1]), "v"(d[2]), "v"(d[3]));
        if (SSP && w < 4) {
          u32 spin = 0;
          do sok = stats(); while (!sok && ++spin < SSP);
        }
      }
      STAMP(9);
      PAD(4, RF("retf"));
      bar();
      if (w < 4) {
        const v4f* p = (const v4f*)(lds + L_G3 + gsel);
        const v4f gp = p[0], gq = p[64];
        if (SSP == 0) stats(); else if (__builtin_expect(!sok, 0)) stats();
        const v4f gs = gp + gq;
        const v2f gh = v2f{gs.x, gs.y} + v2f{gs.z, gs.w};
        const u32 val = asu(rbf(fmaf(fmaf(-mean, S12.x, gh.x + gh.y), ri, S12.y)));
        const float gt = asf(dppm<ROR8, 0xf, 0xc>(val, val)), uu = asf(dppm<ROR8, 0xf, 0x3>(val, val));
        constexpr float GC = -2.f * 0.7978845608028654f * 1.4426950408889634f;
        const float z = gt * fmaf(gt * gt, 0.044715f * GC, GC);
        const float ge = rbf(gt * __builtin_amdgcn_rcpf(1.f + __builtin_amdgcn_exp2f(z)));
        const u32 hh = dup(ge * uu);
        const v4u A0 = MD & dppu<QP(0, 0, 0, 0)>(hh), A1 = MD & dppu<QP(1, 1, 1, 1)>(hh), A2 = MD & dppu<QP(2, 2, 2, 2)>(hh), A3 = MD & dppu<QP(3, 3, 3, 3)>(hh);
        const v4f X = mfma32(A3, wd[3], mfma32(A2, wd[2], mfma32(A1, wd[1], mfma32(A0, wd[0], Z4))));
        const v4f Y = mfma32(A3, wd[7], mfma32(A2, wd[6], mfma32(A1, wd[5], mfma32(A0, wd[4], Z4))));
        auto x0 = __builtin_amdgcn_permlane16_swap(asu(X.x), asu(X.y), false, false), x1 = __builtin_amdgcn_permlane16_swap(asu(X.z), asu(X.w), false, false);
        auto y0 = __builtin_amdgcn_permlane16_swap(asu(Y.x), asu(Y.y), false, false), y1 = __builtin_amdgcn_permlane16_swap(asu(Y.z), asu(Y.w), false, false);
        float s0 = asf(x0[0]) + dppf<ROR8>(asf(x0[1])), s1 = asf(x1[0]) + dppf<ROR8>(asf(x1[1]));
        asm("s_nop 1\n\tv_add_f32_dpp %0, %2, %3 row_ror:8 row_mask:0xf bank_mask:0xc\n\tv_add_f32_dpp %1, %4, %5 row_ror:8 row_mask:0xf bank_mask:0xc"
            : "+v"(s0), "+v"(s1) : "v"(y0[0]), "v"(y0[1]), "v"(y1[0]), "v"(y1[1]));
#pragma unroll
        for (int q = 0; q <= C5; ++q) st64(ws, v2u{enc(s0, TP, tm), enc(s1, TP, tm)}, vd, sd + q * WS_CS);
      }
      STAMP(10);
      cfence();
      if (KEEP4) keep<4, N4>(x4);
      POUT();
      END();
    }
  }
}

ROLE(role_h) {
  asm volatile("s_branch 1f\n" LP "gt2:\n\ts_load_dwordx8 s[16:23], s[4:5], s9\n\t"
               "s_barrier\n"
               "6:\n\ts_waitcnt lgkmcnt(0)\n\ts_memrealtime s[10:11]\n\ts_movreld_b32 s16, s6\n\ts_addk_i32 s9, 128\n\ts_waitcnt lgkmcnt(0)\n\t"
               "s_add_u32 s10, s10, 8\n\t"
               "s_sub_u32 s16, s10, s16\n\ts_sub_u32 s17, s10, s17\n\ts_sub_u32 s18, s10, s18\n\ts_sub_u32 s19, s10, s19\n\ts_sub_u32 s20, s10, s20\n\ts_sub_u32 s21, s10, s21\n\ts_sub_u32 s22, s10, s22\n\ts_sub_u32 s23, s10, s23\n\t"
               "s_max_u32 s12, s16, s17\n\ts_min_u32 s13, s16, s17\n\ts_max_u32 s12, s12, s18\n\ts_min_u32 s13, s13, s18\n\ts_max_u32 s12, s12, s19\n\ts_min_u32 s13, s13, s19\n\ts_max_u32 s12, s12, s20\n\ts_min_u32 s13, s13, s20\n\ts_max_u32 s12, s12, s21\n\ts_min_u32 s13, s13, s21\n\ts_max_u32 s12, s12, s22\n\ts_min_u32 s13, s13, s22\n\ts_max_u32 s12, s12, s23\n\ts_min_u32 s13, s13, s23\n\t"
               "s_cmpk_le_u32 s12, %1\n\ts_cbranch_scc1 7f\n\ts_cmpk_ge_u32 s9, %0 + 2048\n\ts_cbranch_scc1 7f\n\t"
               "s_load_dwordx8 s[16:23], s[4:5], s9\n\ts_branch 6b\n"
               "7:\n\ts_sub_u32 s10, s10, s13\n\ts_or_b32 s10, s10, 1\n\tv_mov_b32 v143, s10\n\tds_write_b32 v145, v143 offset:%2\n\t"
               "s_waitcnt vmcnt(0) lgkmcnt(0)\n\ts_mov_b64 exec, 0\n\ts_branch " LP "sp1\n"
               ".subsection 4\n1:" :: "n"(WS_T0), "n"(GTW), "n"(L_TR), "n"(PT), "n"(PTB - 8 * PT * 64), "n"(PTD),
               "n"(WS_H3), "n"(WS_H4), "n"(WS_ST), "n"(CSX ? (N3 > N4 ? N3 : N4) + 1 : 1), "n"(WS_CS));
  PROLOG();
  IDS(TOFF);
  {
    EPOCH();
    {
      COMMON();
      STAMP(1);
      v4u y[4];
      u32 vo[4], so = WS_H2 + xcd * 4096 + (w - 8) * 1024, ql = L_Q + (w - 8) * 256 + l * 4;
#pragma unroll
      for (int k = 0; k < 4; ++k) { vo[k] = vox + k * 32768; asm volatile("" : "+v"(vo[k])); }
      asm volatile("" : "+s"(so), "+v"(ql));
      u32 tv = 0;
      if (TLBT) {
        const int e = (j - 16) * 4 + w - 8;
        if (e < 5) tv = ld32<AUX_NT>(mk(ARG(A_OUT + e), e == 0 ? 1024 : e == 1 ? 4096 * 1024 : 1792 * 1024), l << 18, 0);
        cfence();
      }
      if (PW & 1) {
        const int i = j - 16;
        const u32 nT = ~T;
        until(T_PWK);
        cfence();
#pragma unroll
        for (int q = 0; q <= (CSX ? N3 : 0); ++q) {
          st64(ws, v2u{nT, nT}, l * 8, WS_H3 + (xcd * 16 + i) * 2048 + (w - 8) * 512 + q * WS_CS);
          if (w == 9) st128(ws, v4u{nT, nT, nT, nT}, (l & 39) ? OOB : l * 32, WS_ST + xcd * 1024 + i * 16 + q * WS_CS);
        }
#pragma unroll
        for (int q = 0; q <= (CSX ? N4 : 0); ++q) if (w == 8) st32(ws, nT, l * 4, WS_H4 + (2 * xcd + (i >> 3)) * 2048 + (i & 7) * 256 + q * WS_CS);
        cfence();
      }
      until(T_TK);
      TOUCH("reth", HOP(8, 16) HOP(9, 12) HOP(10, 11) HOP(11, 13));
      STAMP(4);
      if (GT) {
        u32 age, tn, spin = 0;
        do {
          cfence();
          const u32 t = ld32<AUX_PL>(ws, (l & 7) * 4, WS_T0 + (blk & 15) * 128);
          asm volatile("" :: "v"(t));
          tn = now() + 8;
          age = tn - t;
        } while (__builtin_amdgcn_ballot_w64(age > GTW) != 0 && ++spin < 16);
        age = min(age, dppu<QP(1, 0, 3, 2)>(age)); age = min(age, dppu<QP(2, 3, 0, 1)>(age)); age = min(age, dppu<HMIR>(age));
        age = __builtin_amdgcn_readfirstlane(age);
        if (age <= GTW) t_start = tn - age;
        if (T_H3 && w == 8) *(u32*)(lds + L_TK) = t_start;
      }
      until(T_Q);
      STAMP(2);
      gather<4, true, 7>(y, ws, vo, so, T);
      const v4f s = (asf4(y[0]) + asf4(y[1])) + (asf4(y[2]) + asf4(y[3]));
      *(u32*)(lds + ql) = cvtpk(s.x, s.z);
      asm volatile("" :: "v"(tv));
      STAMP(3);
      if (PW & 1) vmw<0>();
      bar();
      if (WOPF == 2) {
        until(T_PW);
        cfence();
        const u32 pf = ld32<AUX_PF>(mk(ARG(A_WO), 4096 * 512 * 2), l * 1024, (256 * (2 * xcd + ((j - 16) >> 3)) + 64 * (w - 8)) * 1024 + (j & 7) * 128);
        asm volatile("" :: "v"(pf));
      }
      asm volatile("s_endpgm\n\t.subsection 5\n" LP "end:\n\t.subsection 4" ::: "memory");
      return;
    }
  }
}

extern "C" void kernel_launch(void* const* d_in, const int* in_sizes, int n_in, void* d_out, int out_size, void* d_ws, size_t ws_size,
                              hipStream_t stream) {
  (void)in_sizes; (void)n_in; (void)out_size; (void)ws_size;
  fused<<<256, 768, 0, stream>>>((const u16*)d_in[0], (const u16*)d_in[1], (const u16*)d_in[2], (const u16*)d_in[3],
                                 (const u16*)d_in[9], (const u16*)d_in[10], (char*)d_ws, (u16*)d_out,
                                 (const u16*)d_in[4], (const u16*)d_in[6], (const u16*)d_in[7], (const u16*)d_in[8]);
}
```
